```python
import torch

def _softmax(x, axis=-1):
    return torch.softmax(x, dim=axis)

def _gelu(x):
    return torch.nn.functional.gelu(x, approximate='tanh')

def _silu(x):
    return torch.nn.functional.silu(x)

def _sigmoid(x):
    return torch.sigmoid(x)

def _layernorm(x, axis=-1, eps=1e-05):
    m = x.mean(dim=axis, keepdim=True)
    v = x.var(dim=axis, keepdim=True, unbiased=False)
    return (x - m) / torch.sqrt(v + eps)

def _scatter_add(tgt, idx, src, axis=0):
    return tgt.clone().index_add_(axis, idx, src)

def _segment_sum(src, seg, n):
    out = torch.zeros((n,) + src.shape[1:], dtype=src.dtype)
    return out.index_add_(0, seg, src)

def _pad(x, pw):
    flat = tuple((v for pair in reversed(pw) for v in pair))
    return torch.nn.functional.pad(x, flat)
kernel_name = 'standalone__window_attn_twins__vision'

def setup_inputs():

    _w = lambda a: (lambda t: t.to(torch.bfloat16) if t.is_floating_point() else t)(torch.as_tensor(a))
    H = W = 224 // 8 * 8
    C = 96
    return {'x': _w((lambda *s: torch.randn(*s, dtype=torch.bfloat16) * 0.8)(1, H, W, C)), 'wq': _w((lambda *s: torch.randn(*s, dtype=torch.bfloat16) * 0.8)(C, C)), 'wkv': _w((lambda *s: torch.randn(*s, dtype=torch.bfloat16) * 0.8)(C, 2 * C)), 'wo': _w((lambda *s: torch.randn(*s, dtype=torch.bfloat16) * 0.05)(C, C))}

def reference(x, wq, wkv, wo):
    B, H, W, C = x.shape
    win = 8
    nh, nw = (H // win, W // win)
    xs = x.reshape(B, nh, win, nw, win, C).mean(axis=(2, 4)).reshape(B, nh * nw, C)
    q = x.reshape(B, H * W, C) @ wq
    kv = xs @ wkv
    k, v = (kv[..., :C].clone(), kv[..., C:].clone())
    scale = C ** (-0.5)
    p = _softmax(torch.einsum('bqc,bkc->bqk', q, k) * scale, axis=-1)
    o = torch.einsum('bqk,bkc->bqc', p, v) @ wo
    return o.reshape(B, H, W, C)
```

```cpp
#include <hip/hip_runtime.h>
#include <hip/hip_bf16.h>
#include <stdint.h>

typedef __attribute__((ext_vector_type(8))) short bf16x8;
typedef __attribute__((ext_vector_type(16))) float f32x16;
typedef __attribute__((ext_vector_type(4))) unsigned u32x4;
typedef __attribute__((ext_vector_type(2))) unsigned u32x2;
typedef __attribute__((address_space(3))) const char* lds_cptr;
typedef __attribute__((address_space(3))) char* lds_ptr;

constexpr int CH = 96, IMW = 224, NWIN = 28;
constexpr int TILEB = 12288;
constexpr float QSCALE = 0.10206207261596577f * 1.4426950408889634f;

#define MFMA(a, b, c) __builtin_amdgcn_mfma_f32_32x32x16_bf16(a, b, c, 0, 0, 0)
#define SBAR() __builtin_amdgcn_sched_barrier(0)
#define PIN(x) asm volatile("" : "+v"(x))

typedef __attribute__((ext_vector_type(2))) float f32x2;
typedef __attribute__((ext_vector_type(2))) __bf16 bf16x2;
__device__ __forceinline__ unsigned cvtpk(float lo, float hi) { return __builtin_bit_cast(unsigned, __builtin_convertvector((f32x2){lo, hi}, bf16x2)); }
__device__ __forceinline__ int swap23(int r) { return (r & 19) | ((r & 4) << 1) | ((r & 8) >> 1); }
__device__ __forceinline__ bf16x8 pack8(const f32x16& a, int o) {
    u32x4 r = {cvtpk(a[o], a[o + 1]), cvtpk(a[o + 2], a[o + 3]), cvtpk(a[o + 4], a[o + 5]), cvtpk(a[o + 6], a[o + 7])};
    return __builtin_bit_cast(bf16x8, r); }
__device__ __forceinline__ float add_x32(float v) { auto r = __builtin_amdgcn_permlane32_swap(__float_as_uint(v), __float_as_uint(v), false, false); return __uint_as_float(r[0]) + __uint_as_float(r[1]); }
__device__ __forceinline__ float add_x16(float v) { auto r = __builtin_amdgcn_permlane16_swap(__float_as_uint(v), __float_as_uint(v), false, false); return __uint_as_float(r[0]) + __uint_as_float(r[1]); }
__device__ __forceinline__ float max_x32(float v) { auto r = __builtin_amdgcn_permlane32_swap(__float_as_uint(v), __float_as_uint(v), false, false); return __builtin_fmaxf(__uint_as_float(r[0]), __uint_as_float(r[1])); }
__device__ __forceinline__ bf16x8 onehot8(int idx) {
    u32x4 r;
#pragma unroll
    for (int i = 0; i < 4; ++i) r[i] = (idx == 2 * i ? 0x3F80u : 0u) | (idx == 2 * i + 1 ? 0x3F800000u : 0u);
    return __builtin_bit_cast(bf16x8, r); }

__global__ __launch_bounds__(640) void prep_kernel(const uint16_t* __restrict__ x, const uint16_t* __restrict__ wq, const uint16_t* __restrict__ wkv,
                                                   const uint16_t* __restrict__ wo, char* __restrict__ img) {
    __shared__ __attribute__((aligned(16))) uint16_t s_xs[4 * CH];
    __shared__ __attribute__((aligned(16))) uint16_t s_kv[2 * 4 * CH];
    const int b = blockIdx.x, lane = threadIdx.x & 63, wid = __builtin_amdgcn_readfirstlane(threadIdx.x >> 6);
    const int r = lane & 31, h = lane >> 5;
    const f32x16 zero16 = {};
    bf16x8 w1[6], w2[6];
    const int p = (wid - 4) / 3, t = (wid - 4) % 3;
    if (wid < 4) {
        const int wr = (4 * b) / NWIN, wc = (4 * b) % NWIN + wid, g = lane & 15, pp = lane >> 4;
        const uint16_t* src = x + ((size_t)(8 * wr) * IMW + 8 * wc + pp) * CH + 8 * (g < 12 ? g : 0);
        u32x4 v[16];
#pragma unroll
        for (int i = 0; i < 16; ++i) v[i] = *(const u32x4*)(src + ((i >> 1) * IMW + 4 * (i & 1)) * CH);
        float s[8] = {};
#pragma unroll
        for (int i = 0; i < 16; ++i)
#pragma unroll
            for (int c = 0; c < 4; ++c) { s[2 * c] += __uint_as_float(v[i][c] << 16); s[2 * c + 1] += __uint_as_float(v[i][c] & 0xFFFF0000u); }
#pragma unroll
        for (int c = 0; c < 8; ++c) s[c] = add_x32(add_x16(s[c])) * (1.0f / 64.0f);
        if (lane < 12) *(u32x4*)(s_xs + wid * CH + 8 * lane) = (u32x4){cvtpk(s[0], s[1]), cvtpk(s[2], s[3]), cvtpk(s[4], s[5]), cvtpk(s[6], s[7])};
    } else {
        const int sr = swap23(r);
        bf16x8 a1[6], a2[6];
#pragma unroll
        for (int i = 0; i < 6; ++i) a1[i] = *(const bf16x8*)(wkv + (32 * (i >> 1) + sr) * (2 * CH) + CH * p + 32 * t + 16 * (i & 1) + 8 * h);
        const int hp = (r >> 2) & 1, g = (r & 3) + 4 * (r >> 3);
        if (p == 0) {
            const int row = 32 * t + 16 * (g >> 3) + 8 * hp + (g & 7);
#pragma unroll
            for (int i = 0; i < 6; ++i) w2[i] = *(const bf16x8*)(wq + row * CH + 16 * i + 8 * h);
        } else {
#pragma unroll
            for (int i = 0; i < 6; ++i) a2[i] = *(const bf16x8*)(wo + (32 * (i >> 1) + sr) * CH + 48 * (i & 1) + 16 * t + 8 * h);
        }
        const bf16x8 p0 = onehot8(sr - 8 * h), p1 = onehot8(sr - 16 - 8 * h);
#pragma unroll
        for (int c = 0; c < 3; ++c) { f32x16 X = MFMA(a1[2 * c], p0, zero16); X = MFMA(a1[2 * c + 1], p1, X); w1[2 * c] = pack8(X, 0); w1[2 * c + 1] = pack8(X, 8); }
        if (p == 1) {
            const bf16x8 s0 = onehot8(hp == 0 ? g - 8 * h : -1), s1 = onehot8(hp == 1 ? g - 8 * h : -1);
#pragma unroll
            for (int c = 0; c < 3; ++c) { f32x16 X = MFMA(a2[2 * c], s0, zero16); X = MFMA(a2[2 * c + 1], s1, X); w2[2 * c] = pack8(X, 0); w2[2 * c + 1] = pack8(X, 8); }
        }
    }
    __syncthreads();
    if (wid >= 4) {
        f32x16 d = zero16;
#pragma unroll
        for (int i = 0; i < 6; ++i) d = MFMA(w1[i], *(const bf16x8*)(s_xs + (r & 3) * CH + 16 * i + 8 * h), d);
        if (r < 4) {
            *(bf16x8*)(s_kv + (p * 4 + r) * CH + 32 * t + 8 * h) = pack8(d, 0);
            *(bf16x8*)(s_kv + (p * 4 + r) * CH + 32 * t + 16 + 8 * h) = pack8(d, 8);
        }
    }
    __syncthreads();
    if (wid >= 4) {
        f32x16 d = zero16;
        char* tile = img + (size_t)(b >> 3) * TILEB;
        if (p == 0) {
#pragma unroll
            for (int i = 0; i < 6; ++i) d = MFMA(w2[i], *(const bf16x8*)(s_kv + (r & 3) * CH + 16 * i + 8 * h), d);
#pragma unroll
            for (int i = 0; i < 16; ++i) d[i] *= QSCALE;
            if (r < 4) {
                const int kr = (4 * b + r) & 31;
                *(bf16x8*)(tile + (2 * t) * 1024 + (h * 32 + kr) * 16) = pack8(d, 0);
                *(bf16x8*)(tile + (2 * t + 1) * 1024 + (h * 32 + kr) * 16) = pack8(d, 8);
            } else if (b == 195 && r >= 16) {
                *(u32x4*)(tile + (2 * t) * 1024 + lane * 16) = (u32x4){0, 0, 0, 0};
                *(u32x4*)(tile + (2 * t + 1) * 1024 + lane * 16) = (u32x4){0, 0, 0, 0};
            }
        } else {
#pragma unroll
            for (int i = 0; i < 6; ++i) d = MFMA(*(const bf16x8*)(s_kv + (4 + (r & 3)) * CH + 16 * i + 8 * h), w2[i], d);
            if (h == 0) {
                const int g = b & 7;
                *(u32x2*)(tile + 6144 + ((g >> 2) * 3 + t) * 1024 + ((g & 1) * 32 + r) * 16 + ((g >> 1) & 1) * 8) = (u32x2){cvtpk(d[0], d[1]), cvtpk(d[2], d[3])};
            }
        }
    }
}

constexpr int PHB = 2 * TILEB, NSLOT = 6, LDSB = NSLOT * PHB;
__device__ __forceinline__ void glds16(const void* g, unsigned lds_base) {
    unsigned sv; asm volatile("s_mov_b32 %0, m0\n\ts_mov_b32 m0, %2\n\ts_nop 0\n\tglobal_load_lds_dwordx4 %1, off\n\ts_mov_b32 m0, %0" : "=&s"(sv) : "v"(g), "s"(lds_base) : "memory"); }
#define WAIT_BAR(N) asm volatile("s_waitcnt vmcnt(" #N ") lgkmcnt(0)\n\ts_barrier" ::: "memory")
#define LDF(p, off) (*(const __attribute__((address_space(3))) bf16x8*)((p) + (off)))
#define EX2(v) __builtin_amdgcn_exp2f(v)
#define MX3(a, b, c) __builtin_fmaxf(__builtin_fmaxf((a), (b)), (c))

__global__ __launch_bounds__(512, 2) void attn_kernel(const uint16_t* __restrict__ x, const char* __restrict__ img, uint16_t* __restrict__ out) {
    extern __shared__ __attribute__((aligned(16))) char lds[];
    const int b = blockIdx.x, lane = threadIdx.x & 63, wid = __builtin_amdgcn_readfirstlane(threadIdx.x >> 6);
    const int r = lane & 31, h = lane >> 5;
    const int wr = (4 * b) / NWIN, wc = (4 * b) % NWIN;
    const size_t row0 = (size_t)(8 * wr + wid) * IMW + 8 * wc;
    const unsigned lds0 = (unsigned)(uintptr_t)lds;
    bf16x8 xf[6];
    { const uint16_t* xp = x + (row0 + r) * CH + 8 * h;
#pragma unroll
      for (int i = 0; i < 6; ++i) asm volatile("global_load_dwordx4 %0, %1, off offset:%2" : "=v"(xf[i]) : "v"(xp), "i"(32 * i) : "memory"); }
    const char* gsrc = img + wid * 3072 + lane * 16;
    const unsigned ldst = lds0 + wid * 3072;
#define DMA(ph, slot) do { _Pragma("unroll") for (int i_ = 0; i_ < 3; ++i_) glds16(gsrc + (size_t)(ph) * PHB + i_ * 1024, ldst + (slot) + i_ * 1024); } while (0)
    DMA(0, 0); DMA(1, PHB); DMA(2, 2 * PHB); DMA(3, 3 * PHB);
    const lds_cptr lp = (lds_cptr)lds + lane * 16;
    asm volatile("s_waitcnt vmcnt(9)\n\ts_barrier" : "+v"(xf[0]), "+v"(xf[1]), "+v"(xf[2]), "+v"(xf[3]), "+v"(xf[4]), "+v"(xf[5]) :: "memory");
    f32x16 sA, sB, negm, o0 = {}, o1 = {}, o2 = {};
    u32x4 pA0, pA1, pB0, pB1;
    float l0 = 0.f, l1 = 0.f;
    { const f32x16 zero16 = {};
      sA = MFMA(LDF(lp, 0), xf[0], zero16);
#pragma unroll
      for (int i = 1; i < 6; ++i) sA = MFMA(LDF(lp, i * 1024), xf[i], sA);
      float a = MX3(sA[0], sA[1], sA[2]), c = MX3(sA[3], sA[4], sA[5]);
      a = MX3(a, sA[6], sA[7]); c = MX3(c, sA[8], sA[9]); a = MX3(a, sA[10], sA[11]); c = MX3(c, sA[12], sA[13]); a = MX3(a, sA[14], sA[15]);
      const float m = max_x32(__builtin_fmaxf(a, c));
#pragma unroll
      for (int i = 0; i < 16; ++i) { negm[i] = -m; sA[i] -= m; } }
    PIN(negm);
    WAIT_BAR(6);
    bf16x8 f0, f1, f2, f3;
#define PB(P) __builtin_bit_cast(bf16x8, P)
#define E(S, i) S[i] = EX2(S[i])
#define GAP(MF, LD, W) do { MF; LD; W; SBAR(); } while (0)
#define STEP(SC, SN, PC0, PC1, PP0, PP1, kp, vp, kn, DOPV) do { \
    GAP(SN = MFMA(f0, xf[0], negm), f0 = LDF(kp, 4096),                  E(SC, 0); E(SC, 1); PIN(SC)); \
    GAP(SN = MFMA(f1, xf[1], SN),   f1 = LDF(kp, 5120),                  E(SC, 2); l0 += SC[0]; l1 += SC[1]; PC0[0] = cvtpk(SC[0], SC[1]); PIN(SC); PIN(l0); PIN(l1); PIN(PC0)); \
    GAP(SN = MFMA(f2, xf[2], SN),   if (DOPV) f2 = LDF(vp, 6144),        E(SC, 3); E(SC, 4); l0 += SC[2]; PIN(SC); PIN(l0)); \
    GAP(SN = MFMA(f3, xf[3], SN),   if (DOPV) f3 = LDF(vp, 7168),        E(SC, 5); l1 += SC[3]; l0 += SC[4]; PC0[1] = cvtpk(SC[2], SC[3]); PIN(SC); PIN(l0); PIN(l1); PIN(PC0)); \
    GAP(SN = MFMA(f0, xf[4], SN),   if (DOPV) f0 = LDF(vp, 8192),        E(SC, 6); l1 += SC[5]; PC0[2] = cvtpk(SC[4], SC[5]); PIN(SC); PIN(l1); PIN(PC0)); \
    GAP(SN = MFMA(f1, xf[5], SN),   if (DOPV) f1 = LDF(vp, 9216),        E(SC, 7); E(SC, 8); l0 += SC[6]; PIN(SC); PIN(l0)); \
    GAP(if (DOPV) o0 = MFMA(f2, PB(PP0), o0), if (DOPV) f2 = LDF(vp, 10240), E(SC, 9); l1 += SC[7]; l0 += SC[8]; PC0[3] = cvtpk(SC[6], SC[7]); PIN(SC); PIN(l0); PIN(l1); PIN(PC0)); \
    GAP(if (DOPV) o1 = MFMA(f3, PB(PP0), o1), if (DOPV) f3 = LDF(vp, 11264), E(SC, 10); l1 += SC[9]; PC1[0] = cvtpk(SC[8], SC[9]); PIN(SC); PIN(l1); PIN(PC1)); \
    GAP(if (DOPV) o2 = MFMA(f0, PB(PP0), o2), f0 = LDF(kn, 0),           E(SC, 11); E(SC, 12); l0 += SC[10]; PIN(SC); PIN(l0)); \
    GAP(if (DOPV) o0 = MFMA(f1, PB(PP1), o0), f1 = LDF(kn, 1024),        E(SC, 13); l1 += SC[11]; l0 += SC[12]; PC1[1] = cvtpk(SC[10], SC[11]); PIN(SC); PIN(l0); PIN(l1); PIN(PC1)); \
    GAP(if (DOPV) o1 = MFMA(f2, PB(PP1), o1), f2 = LDF(kn, 2048),        E(SC, 14); E(SC, 15); l1 += SC[13]; PIN(SC); PIN(l1)); \
    GAP(if (DOPV) o2 = MFMA(f3, PB(PP1), o2), f3 = LDF(kn, 3072),        PC1[2] = cvtpk(SC[12], SC[13]); l0 += SC[14]; l1 += SC[15]; PC1[3] = cvtpk(SC[14], SC[15]); PIN(l0); PIN(l1); PIN(PC1)); \
    } while (0)
    f0 = LDF(lp, TILEB); f1 = LDF(lp, TILEB + 1024); f2 = LDF(lp, TILEB + 2048); f3 = LDF(lp, TILEB + 3072);
    DMA(4, 4 * PHB);
    STEP(sA, sB, pA0, pA1, pB0, pB1, lp + TILEB, lp, lp + PHB, false);
    STEP(sB, sA, pB0, pB1, pA0, pA1, lp + PHB, lp, lp + PHB + TILEB, true);
    WAIT_BAR(6);
    int sp = 0, sc = PHB, sn = 2 * PHB, sd = 5 * PHB;
    for (int p = 1; p < 12; ++p) {
        if (p < 8) { DMA(p + 4, sd); } else if (p == 8 && wid < 3) { DMA(12, sd); }
        const lds_cptr ap = lp + sp, ac = lp + sc, an = lp + sn;
        STEP(sA, sB, pA0, pA1, pB0, pB1, ac + TILEB, ap + TILEB, an, true);
        STEP(sB, sA, pB0, pB1, pA0, pA1, an, ac, an + TILEB, true);
        if (p < 8) WAIT_BAR(6); else if (p == 8) WAIT_BAR(3); else WAIT_BAR(0);
        sp = sc; sc = sn; sn = (sn == (NSLOT - 1) * PHB) ? 0 : sn + PHB; sd = (sd == (NSLOT - 1) * PHB) ? 0 : sd + PHB;
    }
    { const lds_cptr v23 = lp + 5 * PHB + TILEB + 6144, v24 = lp + 6144;
      o0 = MFMA(LDF(v23, 0), PB(pB0), o0); o1 = MFMA(LDF(v23, 1024), PB(pB0), o1); o2 = MFMA(LDF(v23, 2048), PB(pB0), o2);
      o0 = MFMA(LDF(v23, 3072), PB(pB1), o0); o1 = MFMA(LDF(v23, 4096), PB(pB1), o1); o2 = MFMA(LDF(v23, 5120), PB(pB1), o2);
#pragma unroll
      for (int i = 0; i < 8; ++i) { E(sA, i); if (i & 1) l1 += sA[i]; else l0 += sA[i]; }
      const bf16x8 pl = pack8(sA, 0);
      o0 = MFMA(LDF(v24, 0), pl, o0); o1 = MFMA(LDF(v24, 1024), pl, o1); o2 = MFMA(LDF(v24, 2048), pl, o2); }
    float l = add_x32(l0 + l1);
    if (__builtin_expect(__any(!(l < 1.0e18f)), 0)) {
        const char* gp = img + lane * 16; const f32x16 zero16 = {};
        float m = -INFINITY;
        for (int t = 0; t < 25; ++t) {
            f32x16 s = zero16;
#pragma unroll
            for (int i = 0; i < 6; ++i) s = MFMA(*(const bf16x8*)(gp + (size_t)t * TILEB + i * 1024), xf[i], s);
#pragma unroll
            for (int i = 0; i < 16; ++i) if (i < 8 || t < 24) m = __builtin_fmaxf(m, s[i]);
        }
        m = max_x32(m);
        o0 = zero16; o1 = zero16; o2 = zero16; l = 0.f;
        for (int t = 0; t < 25; ++t) {
            f32x16 s = zero16; const char* tp = gp + (size_t)t * TILEB;
#pragma unroll
            for (int i = 0; i < 6; ++i) s = MFMA(*(const bf16x8*)(tp + i * 1024), xf[i], s);
#pragma unroll
            for (int i = 0; i < 16; ++i) { s[i] = EX2(s[i] - m); if (i < 8 || t < 24) l += s[i]; }
            const bf16x8 q0 = pack8(s, 0), q1 = pack8(s, 8);
            o0 = MFMA(*(const bf16x8*)(tp + 6144), q0, o0); o1 = MFMA(*(const bf16x8*)(tp + 7168), q0, o1); o2 = MFMA(*(const bf16x8*)(tp + 8192), q0, o2);
            if (t < 24) { o0 = MFMA(*(const bf16x8*)(tp + 9216), q1, o0); o1 = MFMA(*(const bf16x8*)(tp + 10240), q1, o1); o2 = MFMA(*(const bf16x8*)(tp + 11264), q1, o2); }
        }
        l = add_x32(l);
    }
    const float rl = 1.0f / l;
#pragma unroll
    for (int i = 0; i < 16; ++i) { o0[i] *= rl; o1[i] *= rl; o2[i] *= rl; }
    { lds_ptr st = (lds_ptr)lds + PHB + wid * (32 * 208);
      lds_ptr wp = st + r * 208 + h * 96;
      typedef __attribute__((address_space(3))) bf16x8* lv;
      *(lv)(wp) = pack8(o0, 0); *(lv)(wp + 16) = pack8(o0, 8); *(lv)(wp + 32) = pack8(o1, 0); *(lv)(wp + 48) = pack8(o1, 8); *(lv)(wp + 64) = pack8(o2, 0); *(lv)(wp + 80) = pack8(o2, 8);
      char* op = (char*)(out + row0 * CH);
#pragma unroll
      for (int i = 0; i < 6; ++i) { const int c = i * 64 + lane; *(bf16x8*)(op + c * 16) = *(lv)(st + (c / 12) * 208 + (c % 12) * 16); } }
}

extern "C" void kernel_launch(void* const* d_in, const int* in_sizes, int n_in, void* d_out, int out_size, void* d_ws, size_t ws_size, hipStream_t stream) {
    const uint16_t* x = (const uint16_t*)d_in[0];
    const uint16_t* wq = (const uint16_t*)d_in[1];
    const uint16_t* wkv = (const uint16_t*)d_in[2];
    const uint16_t* wo = (const uint16_t*)d_in[3];
    char* img = (char*)d_ws;
    (void)hipFuncSetAttribute((const void*)attn_kernel, hipFuncAttributeMaxDynamicSharedMemorySize, LDSB);
    prep_kernel<<<196, 640, 0, stream>>>(x, wq, wkv, wo, img);
    attn_kernel<<<196, 512, LDSB, stream>>>(x, img, (uint16_t*)d_out);
}
```

```cpp
#include <hip/hip_runtime.h>
#include <hip/hip_bf16.h>
#include <stdint.h>

typedef __attribute__((ext_vector_type(8))) short bf16x8;
typedef __attribute__((ext_vector_type(16))) float f32x16;
typedef __attribute__((ext_vector_type(4))) unsigned u32x4;
typedef __attribute__((ext_vector_type(2))) unsigned u32x2;
typedef __attribute__((address_space(3))) const char* lds_cptr;
typedef __attribute__((address_space(3))) char* lds_ptr;

constexpr int GRID1 = 196, GRID2 = 196;
constexpr int CH = 96, IMW = 224, NWIN = 28;
constexpr int TILEB = 12288;
constexpr float QSCALE = 0.10206207261596577f * 1.4426950408889634f;

#define MFMA(a, b, c) __builtin_amdgcn_mfma_f32_32x32x16_bf16(a, b, c, 0, 0, 0)
#define SBAR() __builtin_amdgcn_sched_barrier(0)
#define PIN(x) asm volatile("" : "+v"(x))

typedef __attribute__((ext_vector_type(2))) float f32x2;
typedef __attribute__((ext_vector_type(2))) __bf16 bf16x2;
__device__ __forceinline__ unsigned cvtpk(float lo, float hi) { return __builtin_bit_cast(unsigned, __builtin_convertvector((f32x2){lo, hi}, bf16x2)); }
__device__ __forceinline__ int swap23(int r) { return (r & 19) | ((r & 4) << 1) | ((r & 8) >> 1); }
__device__ __forceinline__ bf16x8 pack8(const f32x16& a, int o) {
    u32x4 r = {cvtpk(a[o], a[o + 1]), cvtpk(a[o + 2], a[o + 3]), cvtpk(a[o + 4], a[o + 5]), cvtpk(a[o + 6], a[o + 7])};
    return __builtin_bit_cast(bf16x8, r); }
__device__ __forceinline__ float add_x32(float v) { auto r = __builtin_amdgcn_permlane32_swap(__float_as_uint(v), __float_as_uint(v), false, false); return __uint_as_float(r[0]) + __uint_as_float(r[1]); }
__device__ __forceinline__ float add_x16(float v) { auto r = __builtin_amdgcn_permlane16_swap(__float_as_uint(v), __float_as_uint(v), false, false); return __uint_as_float(r[0]) + __uint_as_float(r[1]); }
__device__ __forceinline__ float max_x32(float v) { auto r = __builtin_amdgcn_permlane32_swap(__float_as_uint(v), __float_as_uint(v), false, false); return __builtin_fmaxf(__uint_as_float(r[0]), __uint_as_float(r[1])); }
__device__ __forceinline__ bf16x8 onehot8(int idx) {
    u32x4 r;
#pragma unroll
    for (int i = 0; i < 4; ++i) r[i] = (idx == 2 * i ? 0x3F80u : 0u) | (idx == 2 * i + 1 ? 0x3F800000u : 0u);
    return __builtin_bit_cast(bf16x8, r); }

#define STAMP(i)
__global__ __launch_bounds__(640) void prep_kernel(const uint16_t* __restrict__ x, const uint16_t* __restrict__ wq, const uint16_t* __restrict__ wkv,
                                                   const uint16_t* __restrict__ wo, char* __restrict__ img) {
    __shared__ __attribute__((aligned(16))) uint16_t s_xs[4 * CH];
    __shared__ __attribute__((aligned(16))) uint16_t s_kv[2 * 4 * CH];
    const int b = blockIdx.x, lane = threadIdx.x & 63, wid = __builtin_amdgcn_readfirstlane(threadIdx.x >> 6);
    const int r = lane & 31, h = lane >> 5;
#define KID 0
    STAMP(0);
    const f32x16 zero16 = {};
    bf16x8 w1[6], w2[6];
    const int p = (wid - 4) / 3, t = (wid - 4) % 3;
    if (wid < 4) {
        const int wr = (4 * b) / NWIN, wc = (4 * b) % NWIN + wid, g = lane & 15, pp = lane >> 4;
        const uint16_t* src = x + ((size_t)(8 * wr) * IMW + 8 * wc + pp) * CH + 8 * (g < 12 ? g : 0);
        u32x4 v[16];
#pragma unroll
        for (int i = 0; i < 16; ++i) v[i] = *(const u32x4*)(src + ((i >> 1) * IMW + 4 * (i & 1)) * CH);
        float s[8] = {};
#pragma unroll
        for (int i = 0; i < 16; ++i)
#pragma unroll
            for (int c = 0; c < 4; ++c) { s[2 * c] += __uint_as_float(v[i][c] << 16); s[2 * c + 1] += __uint_as_float(v[i][c] & 0xFFFF0000u); }
#pragma unroll
        for (int c = 0; c < 8; ++c) s[c] = add_x32(add_x16(s[c])) * (1.0f / 64.0f);
        if (lane < 12) *(u32x4*)(s_xs + wid * CH + 8 * lane) = (u32x4){cvtpk(s[0], s[1]), cvtpk(s[2], s[3]), cvtpk(s[4], s[5]), cvtpk(s[6], s[7])};
    } else {
        const int sr = swap23(r);
        bf16x8 a1[6], a2[6];
#pragma unroll
        for (int i = 0; i < 6; ++i) a1[i] = *(const bf16x8*)(wkv + (32 * (i >> 1) + sr) * (2 * CH) + CH * p + 32 * t + 16 * (i & 1) + 8 * h);
        const int hp = (r >> 2) & 1, g = (r & 3) + 4 * (r >> 3);
        if (p == 0) {
            const int row = 32 * t + 16 * (g >> 3) + 8 * hp + (g & 7);
#pragma unroll
            for (int i = 0; i < 6; ++i) w2[i] = *(const bf16x8*)(wq + row * CH + 16 * i + 8 * h);
        } else {
#pragma unroll
            for (int i = 0; i < 6; ++i) a2[i] = *(const bf16x8*)(wo + (32 * (i >> 1) + sr) * CH + 48 * (i & 1) + 16 * t + 8 * h);
        }
        const bf16x8 p0 = onehot8(sr - 8 * h), p1 = onehot8(sr - 16 - 8 * h);
#pragma unroll
        for (int c = 0; c < 3; ++c) { f32x16 X = MFMA(a1[2 * c], p0, zero16); X = MFMA(a1[2 * c + 1], p1, X); w1[2 * c] = pack8(X, 0); w1[2 * c + 1] = pack8(X, 8); }
        if (p == 1) {
            const bf16x8 s0 = onehot8(hp == 0 ? g - 8 * h : -1), s1 = onehot8(hp == 1 ? g - 8 * h : -1);
#pragma unroll
            for (int c = 0; c < 3; ++c) { f32x16 X = MFMA(a2[2 * c], s0, zero16); X = MFMA(a2[2 * c + 1], s1, X); w2[2 * c] = pack8(X, 0); w2[2 * c + 1] = pack8(X, 8); }
        }
    }
    STAMP(1);
    __syncthreads();
    STAMP(2);
    if (wid >= 4) {
        f32x16 d = zero16;
#pragma unroll
        for (int i = 0; i < 6; ++i) d = MFMA(w1[i], *(const bf16x8*)(s_xs + (r & 3) * CH + 16 * i + 8 * h), d);
        if (r < 4) {
            *(bf16x8*)(s_kv + (p * 4 + r) * CH + 32 * t + 8 * h) = pack8(d, 0);
            *(bf16x8*)(s_kv + (p * 4 + r) * CH + 32 * t + 16 + 8 * h) = pack8(d, 8);
        }
    }
    __syncthreads();
    if (wid >= 4) {
        f32x16 d = zero16;
        char* tile = img + (size_t)(b >> 3) * TILEB;
        if (p == 0) {
#pragma unroll
            for (int i = 0; i < 6; ++i) d = MFMA(w2[i], *(const bf16x8*)(s_kv + (r & 3) * CH + 16 * i + 8 * h), d);
#pragma unroll
            for (int i = 0; i < 16; ++i) d[i] *= QSCALE;
            if (r < 4) {
                const int kr = (4 * b + r) & 31;
                *(bf16x8*)(tile + (2 * t) * 1024 + (h * 32 + kr) * 16) = pack8(d, 0);
                *(bf16x8*)(tile + (2 * t + 1) * 1024 + (h * 32 + kr) * 16) = pack8(d, 8);
            } else if (b == 195 && r >= 16) {
                *(u32x4*)(tile + (2 * t) * 1024 + lane * 16) = (u32x4){0, 0, 0, 0};
                *(u32x4*)(tile + (2 * t + 1) * 1024 + lane * 16) = (u32x4){0, 0, 0, 0};
            }
        } else {
#pragma unroll
            for (int i = 0; i < 6; ++i) d = MFMA(*(const bf16x8*)(s_kv + (4 + (r & 3)) * CH + 16 * i + 8 * h), w2[i], d);
            if (h == 0) {
                const int g = b & 7;
                *(u32x2*)(tile + 6144 + ((g >> 2) * 3 + t) * 1024 + ((g & 1) * 32 + r) * 16 + ((g >> 1) & 1) * 8) = (u32x2){cvtpk(d[0], d[1]), cvtpk(d[2], d[3])};
            }
        }
    }
    STAMP(3);
#undef KID
}

constexpr int PHB = 2 * TILEB, NSLOT = 6, LDSB = NSLOT * PHB;
__device__ __forceinline__ void glds16(const void* g, unsigned lds_base) {
    unsigned sv; asm volatile("s_mov_b32 %0, m0\n\ts_mov_b32 m0, %2\n\ts_nop 0\n\tglobal_load_lds_dwordx4 %1, off\n\ts_mov_b32 m0, %0" : "=&s"(sv) : "v"(g), "s"(lds_base) : "memory"); }
#define WAIT_BAR(N) asm volatile("s_waitcnt vmcnt(" #N ") lgkmcnt(0)\n\ts_barrier" ::: "memory")
#define LDF(p, off) (*(const __attribute__((address_space(3))) bf16x8*)((p) + (off)))
#define EX2(v) __builtin_amdgcn_exp2f(v)
#define MX3(a, b, c) __builtin_fmaxf(__builtin_fmaxf((a), (b)), (c))

__global__ __launch_bounds__(512, 2) void attn_kernel(const uint16_t* __restrict__ x, const char* __restrict__ img, uint16_t* __restrict__ out) {
    extern __shared__ __attribute__((aligned(16))) char lds[];
    const int b = blockIdx.x, lane = threadIdx.x & 63, wid = __builtin_amdgcn_readfirstlane(threadIdx.x >> 6);
    const int r = lane & 31, h = lane >> 5;
    const int wr = (4 * b) / NWIN, wc = (4 * b) % NWIN;
    const size_t row0 = (size_t)(8 * wr + wid) * IMW + 8 * wc;
    const unsigned lds0 = (unsigned)(uintptr_t)lds;
#define KID 1
    STAMP(0);
    bf16x8 xf[6];
    { const uint16_t* xp = x + (row0 + r) * CH + 8 * h;
#pragma unroll
      for (int i = 0; i < 6; ++i) asm volatile("global_load_dwordx4 %0, %1, off offset:%2" : "=v"(xf[i]) : "v"(xp), "i"(32 * i) : "memory"); }
    const char* gsrc = img + wid * 3072 + lane * 16;
    const unsigned ldst = lds0 + wid * 3072;
#define DMA(ph, slot) do { _Pragma("unroll") for (int i_ = 0; i_ < 3; ++i_) glds16(gsrc + (size_t)(ph) * PHB + i_ * 1024, ldst + (slot) + i_ * 1024); } while (0)
    DMA(0, 0); DMA(1, PHB); DMA(2, 2 * PHB); DMA(3, 3 * PHB);
    const lds_cptr lp = (lds_cptr)lds + lane * 16;
    asm volatile("s_waitcnt vmcnt(9)\n\ts_barrier" : "+v"(xf[0]), "+v"(xf[1]), "+v"(xf[2]), "+v"(xf[3]), "+v"(xf[4]), "+v"(xf[5]) :: "memory");
    STAMP(1);
    f32x16 sA, sB, negm, o0 = {}, o1 = {}, o2 = {};
    u32x4 pA0, pA1, pB0, pB1;
    float l0 = 0.f, l1 = 0.f;
    { const f32x16 zero16 = {};
      sA = MFMA(LDF(lp, 0), xf[0], zero16);
#pragma unroll
      for (int i = 1; i < 6; ++i) sA = MFMA(LDF(lp, i * 1024), xf[i], sA);
      float a = MX3(sA[0], sA[1], sA[2]), c = MX3(sA[3], sA[4], sA[5]);
      a = MX3(a, sA[6], sA[7]); c = MX3(c, sA[8], sA[9]); a = MX3(a, sA[10], sA[11]); c = MX3(c, sA[12], sA[13]); a = MX3(a, sA[14], sA[15]);
      const float m = max_x32(__builtin_fmaxf(a, c));
#pragma unroll
      for (int i = 0; i < 16; ++i) { negm[i] = -m; sA[i] -= m; } }
    PIN(negm);
    WAIT_BAR(6);
    bf16x8 f0, f1, f2, f3;
#define PB(P) __builtin_bit_cast(bf16x8, P)
#define E(S, i) S[i] = EX2(S[i])
#define GAP(MF, LD, W) do { MF; LD; W; SBAR(); } while (0)
#define STEP(SC, SN, PC0, PC1, PP0, PP1, kp, vp, kn, DOPV) do { \
    GAP(SN = MFMA(f0, xf[0], negm), f0 = LDF(kp, 4096),                  E(SC, 0); E(SC, 1); PIN(SC)); \
    GAP(SN = MFMA(f1, xf[1], SN),   f1 = LDF(kp, 5120),                  E(SC, 2); l0 += SC[0]; l1 += SC[1]; PC0[0] = cvtpk(SC[0], SC[1]); PIN(SC); PIN(l0); PIN(l1); PIN(PC0)); \
    GAP(SN = MFMA(f2, xf[2], SN),   if (DOPV) f2 = LDF(vp, 6144),        E(SC, 3); E(SC, 4); l0 += SC[2]; PIN(SC); PIN(l0)); \
    GAP(SN = MFMA(f3, xf[3], SN),   if (DOPV) f3 = LDF(vp, 7168),        E(SC, 5); l1 += SC[3]; l0 += SC[4]; PC0[1] = cvtpk(SC[2], SC[3]); PIN(SC); PIN(l0); PIN(l1); PIN(PC0)); \
    GAP(SN = MFMA(f0, xf[4], SN),   if (DOPV) f0 = LDF(vp, 8192),        E(SC, 6); l1 += SC[5]; PC0[2] = cvtpk(SC[4], SC[5]); PIN(SC); PIN(l1); PIN(PC0)); \
    GAP(SN = MFMA(f1, xf[5], SN),   if (DOPV) f1 = LDF(vp, 9216),        E(SC, 7); E(SC, 8); l0 += SC[6]; PIN(SC); PIN(l0)); \
    GAP(if (DOPV) o0 = MFMA(f2, PB(PP0), o0), if (DOPV) f2 = LDF(vp, 10240), E(SC, 9); l1 += SC[7]; l0 += SC[8]; PC0[3] = cvtpk(SC[6], SC[7]); PIN(SC); PIN(l0); PIN(l1); PIN(PC0)); \
    GAP(if (DOPV) o1 = MFMA(f3, PB(PP0), o1), if (DOPV) f3 = LDF(vp, 11264), E(SC, 10); l1 += SC[9]; PC1[0] = cvtpk(SC[8], SC[9]); PIN(SC); PIN(l1); PIN(PC1)); \
    GAP(if (DOPV) o2 = MFMA(f0, PB(PP0), o2), f0 = LDF(kn, 0),           E(SC, 11); E(SC, 12); l0 += SC[10]; PIN(SC); PIN(l0)); \
    GAP(if (DOPV) o0 = MFMA(f1, PB(PP1), o0), f1 = LDF(kn, 1024),        E(SC, 13); l1 += SC[11]; l0 += SC[12]; PC1[1] = cvtpk(SC[10], SC[11]); PIN(SC); PIN(l0); PIN(l1); PIN(PC1)); \
    GAP(if (DOPV) o1 = MFMA(f2, PB(PP1), o1), f2 = LDF(kn, 2048),        E(SC, 14); E(SC, 15); l1 += SC[13]; PIN(SC); PIN(l1)); \
    GAP(if (DOPV) o2 = MFMA(f3, PB(PP1), o2), f3 = LDF(kn, 3072),        PC1[2] = cvtpk(SC[12], SC[13]); l0 += SC[14]; l1 += SC[15]; PC1[3] = cvtpk(SC[14], SC[15]); PIN(l0); PIN(l1); PIN(PC1)); \
    } while (0)
    f0 = LDF(lp, TILEB); f1 = LDF(lp, TILEB + 1024); f2 = LDF(lp, TILEB + 2048); f3 = LDF(lp, TILEB + 3072);
    DMA(4, 4 * PHB);
    STEP(sA, sB, pA0, pA1, pB0, pB1, lp + TILEB, lp, lp + PHB, false);
    STEP(sB, sA, pB0, pB1, pA0, pA1, lp + PHB, lp, lp + PHB + TILEB, true);
    WAIT_BAR(6);
    STAMP(2);
    int sp = 0, sc = PHB, sn = 2 * PHB, sd = 5 * PHB;
    for (int p = 1; p < 12; ++p) {
        if (p < 8) { DMA(p + 4, sd); } else if (p == 8 && wid < 3) { DMA(12, sd); }
        const lds_cptr ap = lp + sp, ac = lp + sc, an = lp + sn;
        STEP(sA, sB, pA0, pA1, pB0, pB1, ac + TILEB, ap + TILEB, an, true);
        STEP(sB, sA, pB0, pB1, pA0, pA1, an, ac, an + TILEB, true);
        if (p < 8) WAIT_BAR(6); else if (p == 8) WAIT_BAR(3); else WAIT_BAR(0);
        sp = sc; sc = sn; sn = (sn == (NSLOT - 1) * PHB) ? 0 : sn + PHB; sd = (sd == (NSLOT - 1) * PHB) ? 0 : sd + PHB;
    }
    STAMP(3);
    { const lds_cptr v23 = lp + 5 * PHB + TILEB + 6144, v24 = lp + 6144;
      o0 = MFMA(LDF(v23, 0), PB(pB0), o0); o1 = MFMA(LDF(v23, 1024), PB(pB0), o1); o2 = MFMA(LDF(v23, 2048), PB(pB0), o2);
      o0 = MFMA(LDF(v23, 3072), PB(pB1), o0); o1 = MFMA(LDF(v23, 4096), PB(pB1), o1); o2 = MFMA(LDF(v23, 5120), PB(pB1), o2);
#pragma unroll
      for (int i = 0; i < 8; ++i) { E(sA, i); if (i & 1) l1 += sA[i]; else l0 += sA[i]; }
      const bf16x8 pl = pack8(sA, 0);
      o0 = MFMA(LDF(v24, 0), pl, o0); o1 = MFMA(LDF(v24, 1024), pl, o1); o2 = MFMA(LDF(v24, 2048), pl, o2); }
    float l = add_x32(l0 + l1);
    if (__builtin_expect(__any(!(l < 1.0e18f)), 0)) {
        const char* gp = img + lane * 16; const f32x16 zero16 = {};
        float m = -INFINITY;
        for (int t = 0; t < 25; ++t) {
            f32x16 s = zero16;
#pragma unroll
            for (int i = 0; i < 6; ++i) s = MFMA(*(const bf16x8*)(gp + (size_t)t * TILEB + i * 1024), xf[i], s);
#pragma unroll
            for (int i = 0; i < 16; ++i) if (i < 8 || t < 24) m = __builtin_fmaxf(m, s[i]);
        }
        m = max_x32(m);
        o0 = zero16; o1 = zero16; o2 = zero16; l = 0.f;
        for (int t = 0; t < 25; ++t) {
            f32x16 s = zero16; const char* tp = gp + (size_t)t * TILEB;
#pragma unroll
            for (int i = 0; i < 6; ++i) s = MFMA(*(const bf16x8*)(tp + i * 1024), xf[i], s);
#pragma unroll
            for (int i = 0; i < 16; ++i) { s[i] = EX2(s[i] - m); if (i < 8 || t < 24) l += s[i]; }
            const bf16x8 q0 = pack8(s, 0), q1 = pack8(s, 8);
            o0 = MFMA(*(const bf16x8*)(tp + 6144), q0, o0); o1 = MFMA(*(const bf16x8*)(tp + 7168), q0, o1); o2 = MFMA(*(const bf16x8*)(tp + 8192), q0, o2);
            if (t < 24) { o0 = MFMA(*(const bf16x8*)(tp + 9216), q1, o0); o1 = MFMA(*(const bf16x8*)(tp + 10240), q1, o1); o2 = MFMA(*(const bf16x8*)(tp + 11264), q1, o2); }
        }
        l = add_x32(l);
    }
    const float rl = 1.0f / l;
#pragma unroll
    for (int i = 0; i < 16; ++i) { o0[i] *= rl; o1[i] *= rl; o2[i] *= rl; }
    { lds_ptr st = (lds_ptr)lds + PHB + wid * (32 * 208);
      lds_ptr wp = st + r * 208 + h * 96;
      typedef __attribute__((address_space(3))) bf16x8* lv;
      *(lv)(wp) = pack8(o0, 0); *(lv)(wp + 16) = pack8(o0, 8); *(lv)(wp + 32) = pack8(o1, 0); *(lv)(wp + 48) = pack8(o1, 8); *(lv)(wp + 64) = pack8(o2, 0); *(lv)(wp + 80) = pack8(o2, 8);
      char* op = (char*)(out + row0 * CH);
#pragma unroll
      for (int i = 0; i < 6; ++i) { const int c = i * 64 + lane; *(bf16x8*)(op + c * 16) = *(lv)(st + (c / 12) * 208 + (c % 12) * 16); } }
    STAMP(4);
#undef KID
}

extern "C" void kernel_launch(void* const* d_in, const int* in_sizes, int n_in, void* d_out, int out_size, void* d_ws, size_t ws_size, hipStream_t stream) {
    const uint16_t* x = (const uint16_t*)d_in[0];
    const uint16_t* wq = (const uint16_t*)d_in[1];
    const uint16_t* wkv = (const uint16_t*)d_in[2];
    const uint16_t* wo = (const uint16_t*)d_in[3];
    char* img = (char*)d_ws;
    (void)hipFuncSetAttribute((const void*)attn_kernel, hipFuncAttributeMaxDynamicSharedMemorySize, LDSB);
    prep_kernel<<<GRID1, 640, 0, stream>>>(x, wq, wkv, wo, img);
    attn_kernel<<<GRID2, 512, LDSB, stream>>>(x, img, (uint16_t*)d_out);
}
```

```cpp
#define TOUCH 1
#include <hip/hip_runtime.h>
#include <hip/hip_bf16.h>
#include <stdint.h>

typedef __attribute__((ext_vector_type(8))) short bf16x8;
typedef __attribute__((ext_vector_type(4))) short s16x4;
typedef __attribute__((ext_vector_type(16))) float f32x16;
typedef __attribute__((ext_vector_type(4))) float f32x4;
typedef __attribute__((ext_vector_type(2))) float f32x2;
typedef __attribute__((ext_vector_type(2))) __bf16 bf16x2;
typedef __attribute__((ext_vector_type(4))) unsigned u32x4;
typedef __attribute__((ext_vector_type(2))) unsigned u32x2;
#define LAS __attribute__((address_space(3)))
#define GAS __attribute__((address_space(1)))
typedef LAS char* lptr;
typedef GAS unsigned long long gu64;
typedef unsigned long long u64;

constexpr int CH = 96, IMW = 224, NWIN = 28, GRID = 256, NPREP = 196;
constexpr int TILEB = 12288, NSLOT = 13;
constexpr int L_AUX = NSLOT * TILEB, LDSB = 163840;
constexpr int L_X = 73728;
constexpr int CTL_OFF = 1 << 20;
#define NREP 32
constexpr float QSCALE = 0.10206207261596577f * 1.4426950408889634f;

#define MFMA(a, b, c) __builtin_amdgcn_mfma_f32_32x32x16_bf16(a, b, c, 0, 0, 0)
#define MFMA16(a, b, c) __builtin_amdgcn_mfma_f32_16x16x32_bf16(a, b, c, 0, 0, 0)
#define SBAR() __builtin_amdgcn_sched_barrier(0)
#define PIN(x) asm volatile("" : "+v"(x))
#define RLX_AGENT __ATOMIC_RELAXED, __HIP_MEMORY_SCOPE_AGENT
#define WAIT_BAR(N) asm volatile("s_waitcnt vmcnt(" #N ") lgkmcnt(0)\n\ts_barrier" ::: "memory")
#define LBAR() asm volatile("s_waitcnt lgkmcnt(0)\n\ts_barrier" ::: "memory")
#define LDF(p, off) (*(const LAS bf16x8*)((p) + (off)))
#define LDU(p, off) (*(const LAS u32x4*)((p) + (off)))
#define EX2(v) __builtin_amdgcn_exp2f(v)
#define MX3(a, b, c) __builtin_fmaxf(__builtin_fmaxf((a), (b)), (c))
#define PB(P) __builtin_bit_cast(bf16x8, P)
#define ACLOB "a0","a1","a2","a3","a4","a5","a6","a7","a8","a9","a10","a11","a12","a13","a14","a15","a16","a17","a18","a19","a20","a21","a22","a23","a24","a25","a26","a27","a28","a29","a30","a31","a32","a33","a34","a35","a36","a37","a38","a39","a40","a41","a42","a43","a44","a45","a46","a47","a48","a49","a50","a51","a52","a53","a54","a55","a56","a57","a58","a59","a60","a61","a62","a63","a64","a65","a66","a67","a68","a69","a70","a71","a72","a73","a74","a75","a76","a77","a78","a79","a80","a81","a82","a83","a84","a85","a86","a87","a88","a89","a90","a91","a92","a93","a94","a95","a96","a97","a98","a99","a100","a101","a102","a103","a104","a105","a106","a107","a108","a109","a110","a111","a112","a113","a114","a115","a116","a117","a118","a119","a120","a121","a122","a123","a124","a125","a126","a127","a128","a129","a130","a131","a132","a133","a134","a135","a136","a137","a138","a139","a140","a141","a142","a143","a144","a145","a146","a147","a148","a149","a150","a151","a152","a153","a154","a155","a156","a157","a158","a159","a160","a161","a162","a163","a164","a165","a166","a167","a168","a169","a170","a171","a172","a173","a174","a175","a176","a177","a178","a179","a180","a181","a182","a183","a184","a185","a186","a187","a188","a189","a190","a191","a192","a193","a194","a195","a196","a197","a198","a199","a200","a201","a202","a203","a204","a205","a206","a207","a208","a209","a210","a211","a212","a213","a214","a215","a216","a217","a218","a219","a220","a221","a222","a223","a224","a225","a226","a227","a228","a229","a230","a231","a232","a233","a234","a235","a236","a237","a238","a239","a240","a241","a242","a243","a244","a245","a246","a247","a248","a249","a250","a251","a252","a253","a254","a255"
constexpr int OA = 0, OB = 48, OC = 96, OD = 144, XA = 192, XB = 216;

__device__ __forceinline__ unsigned cvtpk(float lo, float hi) { return __builtin_bit_cast(unsigned, __builtin_convertvector((f32x2){lo, hi}, bf16x2)); }
__device__ __forceinline__ bf16x8 pack8(const f32x16& a, int o) {
    u32x4 r = {cvtpk(a[o], a[o + 1]), cvtpk(a[o + 2], a[o + 3]), cvtpk(a[o + 4], a[o + 5]), cvtpk(a[o + 6], a[o + 7])};
    return __builtin_bit_cast(bf16x8, r); }
__device__ __forceinline__ u64 pack4(const f32x4& a) { return (u64)cvtpk(a[0], a[1]) | ((u64)cvtpk(a[2], a[3]) << 32); }
__device__ __forceinline__ float add_x32(float v) { auto r = __builtin_amdgcn_permlane32_swap(__float_as_uint(v), __float_as_uint(v), false, false); return __uint_as_float(r[0]) + __uint_as_float(r[1]); }
__device__ __forceinline__ float add_x16(float v) { auto r = __builtin_amdgcn_permlane16_swap(__float_as_uint(v), __float_as_uint(v), false, false); return __uint_as_float(r[0]) + __uint_as_float(r[1]); }
__device__ __forceinline__ float max_x32(float v) { auto r = __builtin_amdgcn_permlane32_swap(__float_as_uint(v), __float_as_uint(v), false, false); return __builtin_fmaxf(__uint_as_float(r[0]), __uint_as_float(r[1])); }
__device__ __forceinline__ void glds16(const void* g, unsigned lds_base) {
    unsigned sv; asm volatile("s_mov_b32 %0, m0\n\ts_mov_b32 m0, %2\n\ts_nop 0\n\tglobal_load_lds_dwordx4 %1, off\n\ts_mov_b32 m0, %0" : "=&s"(sv) : "v"(g), "s"(lds_base) : "memory"); }
__device__ __forceinline__ bf16x8 trfrag(lptr p, int rowpitch4) {
    typedef short v4i16_t __attribute__((ext_vector_type(4)));
    const s16x4 lo = __builtin_amdgcn_ds_read_tr16_b64_v4i16((LAS v4i16_t*)p), hi = __builtin_amdgcn_ds_read_tr16_b64_v4i16((LAS v4i16_t*)(p + rowpitch4));
    return (bf16x8){lo[0], lo[1], lo[2], lo[3], hi[0], hi[1], hi[2], hi[3]}; }

#define STAMP(i)
#define STAMPJ()

__device__ __forceinline__ void slow_tile(const uint16_t* x, const char* img, uint16_t* out, int row0, int lane) {
    const int r = lane & 31, h = lane >> 5; const char* gp = img + lane * 16; const f32x16 zero16 = {};
    bf16x8 xf[6];
#pragma unroll
    for (int i = 0; i < 6; ++i) xf[i] = *(const bf16x8*)(x + (size_t)(row0 + r) * CH + 16 * i + 8 * h);
    float m = -INFINITY, l = 0.f;
    for (int t = 0; t < 25; ++t) {
        f32x16 s = zero16;
#pragma unroll
        for (int i = 0; i < 6; ++i) s = MFMA(*(const bf16x8*)(gp + (size_t)t * TILEB + i * 1024), xf[i], s);
#pragma unroll
        for (int i = 0; i < 16; ++i) if (i < 8 || t < 24) m = __builtin_fmaxf(m, s[i]);
    }
    m = max_x32(m);
    f32x16 o[3] = {zero16, zero16, zero16};
    for (int t = 0; t < 25; ++t) {
        f32x16 s = zero16; const char* tp = gp + (size_t)t * TILEB;
#pragma unroll
        for (int i = 0; i < 6; ++i) s = MFMA(*(const bf16x8*)(tp + i * 1024), xf[i], s);
#pragma unroll
        for (int i = 0; i < 16; ++i) { s[i] = EX2(s[i] - m); if (i < 8 || t < 24) l += s[i]; }
        const bf16x8 q0 = pack8(s, 0), q1 = pack8(s, 8);
#pragma unroll
        for (int c = 0; c < 3; ++c) o[c] = MFMA(*(const bf16x8*)(tp + 6144 + c * 1024), q0, o[c]);
        if (t < 24) {
#pragma unroll
            for (int c = 0; c < 3; ++c) o[c] = MFMA(*(const bf16x8*)(tp + 9216 + c * 1024), q1, o[c]);
        }
    }
    const float rl = 1.0f / add_x32(l);
#pragma unroll
    for (int c = 0; c < 3; ++c) {
#pragma unroll
        for (int i = 0; i < 16; ++i) o[c][i] *= rl;
        uint16_t* op = out + (size_t)(row0 + r) * CH + 48 * h + 16 * c;
        *(bf16x8*)op = pack8(o[c], 0); *(bf16x8*)(op + 8) = pack8(o[c], 8);
    }
}

template <bool extra>
__device__ __forceinline__ void attend(const uint16_t* __restrict__ x, char* ws, uint16_t* __restrict__ out, const lptr lds, const unsigned lds0, const int blk, const int lane, const int wid, const u64 tag) {
    const int r = lane & 31, h = lane >> 5, grp = wid >> 1, par = wid & 1;
    char* img = ws;
    gu64* ctl = (gu64*)(ws + CTL_OFF);
    const int rowa = 192 * blk + 96 * grp + 64 * par, rowb = 192 * blk + 96 * grp + 32, rowd = 49152 + 32 * (blk >> 3);
    u32x4 xc[6], xd[6];
    { const unsigned pa = lds0 + L_X + (96 * grp + 64 * par + r) * 192 + 16 * h, pb = lds0 + L_X + (96 * grp + 32 + r) * 192 + 16 * h;
#define XLD(i) asm volatile("ds_read_b128 a[%c2:%c3], %0 offset:%c6\n\tds_read_b128 a[%c4:%c5], %1 offset:%c6" :: "v"(pa), "v"(pb), \
          "i"(XA + 4 * i), "i"(XA + 4 * i + 3), "i"(XB + 4 * i), "i"(XB + 4 * i + 3), "i"(32 * i) : "memory", ACLOB)
      XLD(0); XLD(1); XLD(2); XLD(3); XLD(4); XLD(5);
#pragma unroll
      for (int i = 0; i < 6; ++i) { xc[i] = LDU(lds, L_X + (96 * grp + 64 - 64 * par + r) * 192 + 16 * h + 32 * i); PIN(xc[i]);
          if (extra) { xd[i] = LDU(lds, L_X + (192 + r) * 192 + 16 * h + 32 * i); PIN(xd[i]); } }
      u32x4 z = {0, 0, 0, 0};
#define ZO(i) "v_mfma_f32_32x32x16_bf16 a[" #i ":" #i "+15], %0, %0, 0\n\t"
      asm volatile("s_nop 1\n\t" ZO(0) ZO(16) ZO(32) ZO(48) ZO(64) ZO(80) ZO(96) ZO(112) ZO(128) ZO(144) ZO(160) ZO(176) :: "v"(z) : ACLOB); }
    const unsigned vo0 = lane * 16, vo1 = vo0 + 4096;
    const char* gk = img + wid * TILEB;
    unsigned lk = lds0 + wid * TILEB;
#define M0SET(v) asm volatile("s_mov_b32 m0, %0" :: "s"(v) : "memory")
#define DMA(vo, g, off) asm volatile("global_load_lds_dwordx4 %0, %1 offset:" #off " sc1" :: "v"(vo), "s"(g) : "memory")
#define DMA6(g, l) do { M0SET(l); asm volatile("s_nop 0"); DMA(vo0, g, 0); DMA(vo0, g, 1024); DMA(vo0, g, 2048); DMA(vo0, g, 3072); M0SET((l) + 4096); asm volatile("s_nop 0"); DMA(vo1, g, 0); DMA(vo1, g, 1024); } while (0)
    if (wid == 0) {
        const GAS u32x4* fp = (const GAS u32x4*)(ctl + 256 + 256 * (blk & (NREP - 1))) + lane;
        const unsigned tl = (unsigned)tag, th = (unsigned)(tag >> 32);
        for (unsigned spins = 0; spins < (1u << 20); ++spins) {
            u32x4 f0, f1;
            asm volatile("global_load_dwordx4 %0, %2, off sc1\n\tglobal_load_dwordx4 %1, %2, off offset:1024 sc1\n\ts_waitcnt vmcnt(0)" : "=&v"(f0), "=&v"(f1) : "v"(fp) : "memory");
            const unsigned df = ((f0[0] ^ tl) | (f0[1] ^ th) | (f0[2] ^ tl) | (f0[3] ^ th)) | ((f1[0] ^ tl) | (f1[1] ^ th) | (f1[2] ^ tl) | (f1[3] ^ th));
            if (__all(df == 0)) break;
        }
    }
    LBAR();
    STAMP(3);
    DMA6(gk, lk); DMA6(gk + 6144, lk + 6144); DMA6(gk + 4 * TILEB, lk + 4 * TILEB);
    const lptr lp = lds + lane * 16;
    f32x16 sa, sb, sc, sd, negm;
    u32x4 pa0, pa1, pb0, pb1, pc0 = {}, pc1 = {}, pd0, pd1;
    float la = 0.f, lb = 0.f, lc = 0.f, ld = 0.f, lt;
    u32x4 kf[6], vf[6];
    float mw;
    asm volatile("s_waitcnt vmcnt(12)" ::: "memory");
    STAMP(4);
    { const lptr k0 = lp + wid * TILEB;
#pragma unroll
      for (int i = 0; i < 6; ++i) { kf[i] = LDU(k0, i * 1024); vf[i] = (u32x4){0, 0, 0, 0}; }
      asm volatile("v_mfma_f32_32x32x16_bf16 %0, %1, a[192:195], 0\n\tv_mfma_f32_32x32x16_bf16 %0, %2, a[196:199], %0\n\tv_mfma_f32_32x32x16_bf16 %0, %3, a[200:203], %0\n\t"
                   "v_mfma_f32_32x32x16_bf16 %0, %4, a[204:207], %0\n\tv_mfma_f32_32x32x16_bf16 %0, %5, a[208:211], %0\n\tv_mfma_f32_32x32x16_bf16 %0, %6, a[212:215], %0\n\t"
                   "s_barrier\n\ts_nop 15\n\ts_nop 3"
                   : "=&v"(sa) : "v"(kf[0]), "v"(kf[1]), "v"(kf[2]), "v"(kf[3]), "v"(kf[4]), "v"(kf[5]) : "memory", ACLOB);
      float a = MX3(sa[0], sa[1], sa[2]), c = MX3(sa[3], sa[4], sa[5]);
      a = MX3(a, sa[6], sa[7]); c = MX3(c, sa[8], sa[9]); a = MX3(a, sa[10], sa[11]); c = MX3(c, sa[12], sa[13]); a = MX3(a, sa[14], sa[15]);
      const float m = max_x32(__builtin_fmaxf(a, c)); mw = m;
#pragma unroll
      for (int i = 0; i < 16; ++i) { negm[i] = -m; sa[i] -= m; } }
    if (blk == 0 && threadIdx.x == 0) __hip_atomic_store(ctl, tag, RLX_AGENT);
    asm volatile("s_nop 3" : "+v"(negm), "+v"(pc0), "+v"(pc1), "+v"(vf[0]), "+v"(vf[1]), "+v"(vf[2]), "+v"(vf[3]), "+v"(vf[4]), "+v"(vf[5]));
#define E(S, i) S[i] = EX2(S[i])
#define GAP(MF, LD, ...) do { MF; SBAR(); LD; __VA_ARGS__; SBAR(); } while (0)
#define KR(R, i) if (R) kf[i] = LDU(kn, (i) * 1024)
#define VR(R, i) if (R) vf[i] = LDU(vn, 6144 + (i) * 1024)
#define QK0_A(S, i, X) asm volatile("v_mfma_f32_32x32x16_bf16 %0, %1, a[%c3:%c4], %2" : "=&v"(S) : "v"(kf[i]), "v"(negm), "i"(X + 4 * (i)), "i"(X + 4 * (i) + 3) : ACLOB)
#define QKN_A(S, i, X) asm volatile("v_mfma_f32_32x32x16_bf16 %0, %1, a[%c2:%c3], %0" : "+v"(S) : "v"(kf[i]), "i"(X + 4 * (i)), "i"(X + 4 * (i) + 3) : ACLOB)
#define QK0_V(S, i, X) asm volatile("v_mfma_f32_32x32x16_bf16 %0, %1, %2, %3" : "=&v"(S) : "v"(kf[i]), "v"(X[i]), "v"(negm) : ACLOB)
#define QKN_V(S, i, X) asm volatile("v_mfma_f32_32x32x16_bf16 %0, %1, %2, %0" : "+v"(S) : "v"(kf[i]), "v"(X[i]) : ACLOB)
#define QK0(S, i, X, XC) QK0_##XC(S, i, X)
#define QKN(S, i, X, XC) QKN_##XC(S, i, X)
#define PVM(O, i, P) asm volatile("v_mfma_f32_32x32x16_bf16 a[%c2:%c3], %0, %1, a[%c2:%c3]" :: "v"(vf[i]), "v"(P), "i"(O + 16 * ((i) % 3)), "i"(O + 16 * ((i) % 3) + 15) : ACLOB)
#define X1(S, i) do { e_[i] = EX2(S[i]); PIN(e_[i]); } while (0)
#define CV(P, k, i) do { unsigned c_ = cvtpk(e_[i], e_[(i) + 1]); PIN(c_); P[k] = c_; } while (0)
#define AD(L, i) do { L += e_[i]; PIN(L); } while (0)
#define STEP(SQ, XQ, XC, RK, SC, PC0, PC1, L, OP, PP0, PP1, RV, D0, D1) do { float e_[16]; PIN(SC); \
    GAP(QK0(SQ, 0, XQ, XC), KR(RK, 0), X1(SC, 0); X1(SC, 1)); \
    GAP(QKN(SQ, 1, XQ, XC), KR(RK, 1), X1(SC, 2); lt = e_[0] + e_[1]; PIN(lt); CV(PC0, 0, 0); D0); \
    GAP(QKN(SQ, 2, XQ, XC), KR(RK, 2), X1(SC, 3); X1(SC, 4); AD(L, 2)); \
    GAP(QKN(SQ, 3, XQ, XC), KR(RK, 3), X1(SC, 5); AD(lt, 3); AD(L, 4); CV(PC0, 1, 2)); \
    GAP(QKN(SQ, 4, XQ, XC), KR(RK, 4), X1(SC, 6); AD(lt, 5); CV(PC0, 2, 4)); \
    GAP(QKN(SQ, 5, XQ, XC), KR(RK, 5), X1(SC, 7); X1(SC, 8); AD(L, 6)); \
    GAP(PVM(OP, 0, PP0), VR(RV, 0), X1(SC, 9); AD(lt, 7); AD(L, 8); CV(PC0, 3, 6)); \
    GAP(PVM(OP, 1, PP0), VR(RV, 1), X1(SC, 10); AD(lt, 9); CV(PC1, 0, 8); D1); \
    GAP(PVM(OP, 2, PP0), VR(RV, 2), X1(SC, 11); X1(SC, 12); AD(L, 10)); \
    GAP(PVM(OP, 3, PP1), VR(RV, 3), X1(SC, 13); AD(lt, 11); AD(L, 12); CV(PC1, 1, 10)); \
    GAP(PVM(OP, 4, PP1), VR(RV, 4), X1(SC, 14); X1(SC, 15); AD(lt, 13)); \
    GAP(PVM(OP, 5, PP1), VR(RV, 5), CV(PC1, 2, 12); AD(L, 14); AD(lt, 15); CV(PC1, 3, 14); L += lt; PIN(L)); \
    } while (0)
    int so = 0;
#pragma unroll 1
    for (int j = 0; j < 6; ++j) {
        const int s1 = so == 8 * TILEB ? 0 : so + 4 * TILEB, s2 = s1 == 8 * TILEB ? 0 : s1 + 4 * TILEB;
        { const lptr vn = lp + so + wid * TILEB, kn = lp + so + (wid ^ 2) * TILEB;
          const char* g = img + (j == 5 ? 24 : 4 * j + 4 + wid) * TILEB + 6144; const unsigned l = lk + s1 + 6144;
          asm volatile("s_waitcnt vmcnt(6)" ::: "memory"); M0SET(l);
          STEP(sb, XB, A, 0, sa, pa0, pa1, la, OC, pc0, pc1, 1, DMA(vo0, g, 0), DMA(vo0, g, 1024));
          if (extra) {
              STEP(sd, xd, V, 0, sb, pb0, pb1, lb, OA, pa0, pa1, 0, DMA(vo0, g, 2048), DMA(vo0, g, 3072));
              M0SET(l + 4096);
              STEP(sc, xc, V, 1, sd, pd0, pd1, ld, OB, pb0, pb1, 0, DMA(vo1, g, 0), DMA(vo1, g, 1024));
              STEP(sa, XA, A, 0, sc, pc0, pc1, lc, OD, pd0, pd1, 0, , );
          } else {
              STEP(sc, xc, V, 1, sb, pb0, pb1, lb, OA, pa0, pa1, 0, DMA(vo0, g, 2048), DMA(vo0, g, 3072));
              M0SET(l + 4096);
              STEP(sa, XA, A, 0, sc, pc0, pc1, lc, OB, pb0, pb1, 0, DMA(vo1, g, 0), DMA(vo1, g, 1024));
          } }
        STAMPJ();
        { const lptr vn = lp + so + (wid ^ 2) * TILEB, kn = lp + s1 + wid * TILEB;
          const char* g = img + (j >= 4 ? 24 : 4 * j + 8 + wid) * TILEB; const unsigned l = j == 5 ? lds0 + 12 * TILEB : lk + s2;
          asm volatile("s_waitcnt vmcnt(6)\n\ts_barrier" ::: "memory"); M0SET(l);
          STEP(sb, XB, A, 0, sa, pa0, pa1, la, OC, pc0, pc1, 1, DMA(vo0, g, 0), DMA(vo0, g, 1024));
          STEP(sc, xc, V, 1, sb, pb0, pb1, lb, OA, pa0, pa1, 0, DMA(vo0, g, 2048), DMA(vo0, g, 3072));
          M0SET(l + 4096);
          STEP(sa, XA, A, 0, sc, pc0, pc1, lc, OB, pb0, pb1, 0, DMA(vo1, g, 0), DMA(vo1, g, 1024)); }
        so = s1;
    }
    STAMP(5);
    constexpr int X_C = 4 * TILEB, X_B = 8 * TILEB, X_D = 0, X_ST = 8 * TILEB + 6144, X_L = L_AUX;
#define WRA(p, A, off) asm volatile("ds_write_b128 %0, a[%c1:%c2] offset:%c3" :: "v"(p), "i"(A), "i"((A) + 3), "i"(off) : "memory", ACLOB)
#define WRA6(p, A, off) do { WRA(p, A, off); WRA(p, (A) + 4, (off) + 1024); WRA(p, (A) + 8, (off) + 2048); WRA(p, (A) + 12, (off) + 3072); WRA(p, (A) + 16, (off) + 4096); WRA(p, (A) + 20, (off) + 5120); } while (0)
#define RDA(V, A) asm volatile("v_accvgpr_read_b32 %0, a[%c4]\n\tv_accvgpr_read_b32 %1, a[%c4+1]\n\tv_accvgpr_read_b32 %2, a[%c4+2]\n\tv_accvgpr_read_b32 %3, a[%c4+3]" \
        : "=v"(V[0]), "=v"(V[1]), "=v"(V[2]), "=v"(V[3]) : "i"(A) : ACLOB)
#define HALF(S, L, O) do { _Pragma("unroll") for (int i = 0; i < 8; ++i) { E(S, i); L += S[i]; } \
      u32x4 pl_ = {cvtpk(S[0], S[1]), cvtpk(S[2], S[3]), cvtpk(S[4], S[5]), cvtpk(S[6], S[7])}; \
      asm volatile("s_nop 1\n\tv_mfma_f32_32x32x16_bf16 a[%c4:%c4+15], %0, %3, a[%c4:%c4+15]\n\tv_mfma_f32_32x32x16_bf16 a[%c4+16:%c4+31], %1, %3, a[%c4+16:%c4+31]\n\t" \
                   "v_mfma_f32_32x32x16_bf16 a[%c4+32:%c4+47], %2, %3, a[%c4+32:%c4+47]" :: "v"(vf[0]), "v"(vf[1]), "v"(vf[2]), "v"(pl_), "i"(O) : ACLOB); } while (0)
    { const lptr vn = lp + wid * TILEB;
      const unsigned wc_ = lds0 + lane * 16 + X_C + wid * 12288, wb_ = wc_ + (X_B - X_C), wd_ = wc_ - X_C;
      asm volatile("s_waitcnt vmcnt(6)\n\ts_nop 1" : "+v"(pc0), "+v"(pc1), "+v"(vf[0]), "+v"(vf[1]), "+v"(vf[2]), "+v"(vf[3]), "+v"(vf[4]), "+v"(vf[5]));
      PVM(OC, 0, pc0); VR(1, 0); PVM(OC, 1, pc0); VR(1, 1); PVM(OC, 2, pc0); VR(1, 2); PVM(OC, 3, pc1); PVM(OC, 4, pc1); PVM(OC, 5, pc1);
      if (par == 0) {
          asm volatile("s_nop 1\n\tv_mfma_f32_32x32x16_bf16 %0, %1, a[216:219], %7\n\tv_mfma_f32_32x32x16_bf16 %0, %2, a[220:223], %0\n\tv_mfma_f32_32x32x16_bf16 %0, %3, a[224:227], %0\n\t"
                       "v_mfma_f32_32x32x16_bf16 %0, %4, a[228:231], %0\n\tv_mfma_f32_32x32x16_bf16 %0, %5, a[232:235], %0\n\tv_mfma_f32_32x32x16_bf16 %0, %6, a[236:239], %0"
                       : "=&v"(sb) : "v"(kf[0]), "v"(kf[1]), "v"(kf[2]), "v"(kf[3]), "v"(kf[4]), "v"(kf[5]), "v"(negm) : ACLOB);
          WRA6(wc_, OC, 0); HALF(sa, la, OA); PIN(sb); HALF(sb, lb, OB); WRA6(wc_, OC + 24, 6144);
          if (extra) { WRA6(wd_, OD, 0); WRA6(wd_, OD + 24, 6144); } else asm volatile("s_nop 15" ::: ACLOB);
          WRA6(wb_, OB + 24, 0);
      } else {
          WRA6(wb_, OB, 0);
          if (extra && grp == 1) {
              asm volatile("s_nop 1\n\tv_mfma_f32_32x32x16_bf16 %0, %1, %8, %7\n\tv_mfma_f32_32x32x16_bf16 %0, %2, %9, %0\n\tv_mfma_f32_32x32x16_bf16 %0, %3, %10, %0\n\t"
                           "v_mfma_f32_32x32x16_bf16 %0, %4, %11, %0\n\tv_mfma_f32_32x32x16_bf16 %0, %5, %12, %0\n\tv_mfma_f32_32x32x16_bf16 %0, %6, %13, %0"
                           : "=&v"(sd) : "v"(kf[0]), "v"(kf[1]), "v"(kf[2]), "v"(kf[3]), "v"(kf[4]), "v"(kf[5]), "v"(negm), "v"(xd[0]), "v"(xd[1]), "v"(xd[2]), "v"(xd[3]), "v"(xd[4]), "v"(xd[5]) : ACLOB);
              HALF(sa, la, OA); PIN(sd); HALF(sd, ld, OD);
          } else HALF(sa, la, OA);
          WRA6(wc_, OC, 0); WRA6(wc_, OC + 24, 6144);
          if (extra) { WRA6(wd_, OD, 0); WRA6(wd_, OD + 24, 6144); }
      }
      la = add_x32(la); lb = add_x32(lb); ld = add_x32(ld);
      *(LAS f32x4*)(lds + X_L + wid * 1024 + lane * 16) = (f32x4){lb, add_x32(lc), ld, mw}; }
    WAIT_BAR(0);
    STAMP(6);
    const int pw = wid ^ 1;
    typedef LAS u32x4* lu;
    typedef const LAS f32x4* lf;
    const f32x4 lq = *(lf)(lds + X_L + pw * 1024 + lane * 16);
    f32x4 q2, q3, qb[6], qc[12], qd[12];
    const lptr cp = lp + X_C + pw * 12288;
    if (extra) { q2 = *(lf)(lds + X_L + (wid ^ 2) * 1024 + lane * 16); q3 = *(lf)(lds + X_L + (wid ^ 3) * 1024 + lane * 16); }
#pragma unroll
    for (int i = 0; i < 6; ++i) qb[i] = *(lf)(cp + (X_B - X_C) + i * 1024);
#pragma unroll
    for (int i = 0; i < 12; ++i) qc[i] = *(lf)(cp + i * 1024);
    if (extra) {
#pragma unroll
        for (int i = 0; i < 12; ++i) qd[i] = *(lf)(lp + X_D + (wid ^ (i & 3)) * TILEB + (3 * wid + (i >> 2)) * 1024);
    }
    SBAR();
    const float mx = __builtin_fmaxf(mw, lq[3]), fo = EX2(mw - mx), fp_ = EX2(lq[3] - mx);
    const float ta = la * fo + lq[1] * fp_, tb = lb * fo + lq[0] * fp_;
#define LOK(t, u, v, w) __all((t) > 1.0e-18f / w && (u) < 1.0e18f * w && (v) < 1.0e18f * w)
    const bool bada = !LOK(ta, la, lq[1], 1.0f), badb = !LOK(tb, lb, lq[0], 1.0f), okb = LOK(tb, lb, lq[0], 2.0f);
    bool badd = false, okd = false;
#define FIN8(A, e_, f_) ({ float u_[4], w_[4]; RDA(u_, A); RDA(w_, (A) + 4); \
        (u32x4){cvtpk(u_[0] * ro + e_[0] * rp, u_[1] * ro + e_[1] * rp), cvtpk(u_[2] * ro + e_[2] * rp, u_[3] * ro + e_[3] * rp), \
                cvtpk(w_[0] * ro + f_[0] * rp, w_[1] * ro + f_[1] * rp), cvtpk(w_[2] * ro + f_[2] * rp, w_[3] * ro + f_[3] * rp)}; })
    { const float rl = __builtin_amdgcn_rcpf(tb), ro = rl * fo, rp = rl * fp_; const lptr wp = lds + X_ST + grp * TILEB + r * 192 + h * 96;
      if (par == 0) { *(lu)(wp) = FIN8(OB, qb[0], qb[1]); *(lu)(wp + 16) = FIN8(OB + 8, qb[2], qb[3]); *(lu)(wp + 32) = FIN8(OB + 16, qb[4], qb[5]); }
      else { *(lu)(wp + 48) = FIN8(OB + 24, qb[0], qb[1]); *(lu)(wp + 64) = FIN8(OB + 32, qb[2], qb[3]); *(lu)(wp + 80) = FIN8(OB + 40, qb[4], qb[5]); } }
    { const float rl = __builtin_amdgcn_rcpf(ta), ro = rl * fo, rp = rl * fp_;
      const lptr wp = lds + X_C + pw * 12288 + r * 192 + h * 96;
      asm volatile("s_waitcnt lgkmcnt(0)" ::: "memory");
      *(lu)(wp) = FIN8(OA, qc[0], qc[1]); *(lu)(wp + 16) = FIN8(OA + 8, qc[2], qc[3]); *(lu)(wp + 32) = FIN8(OA + 16, qc[4], qc[5]);
      *(lu)(wp + 48) = FIN8(OA + 24, qc[6], qc[7]); *(lu)(wp + 64) = FIN8(OA + 32, qc[8], qc[9]); *(lu)(wp + 80) = FIN8(OA + 40, qc[10], qc[11]); }
    if (extra) {
        const float m4 = MX3(mx, q2[3], q3[3]), f0 = EX2(mw - m4), f1 = EX2(lq[3] - m4), f2 = EX2(q2[3] - m4), f3 = EX2(q3[3] - m4);
        const float td = (ld * f0 + lq[2] * f1) + (q2[2] * f2 + q3[2] * f3), lm = (ld + lq[2]) + (q2[2] + q3[2]);
        badd = !LOK(td, lm, lm, 1.0f); okd = LOK(td, lm, lm, 2.0f);
        const float rl = __builtin_amdgcn_rcpf(td);
        const lptr wp = lds + X_ST + 2 * TILEB + r * 192 + h * 96 + wid * 24;
#pragma unroll
        for (int i = 0; i < 3; ++i) *(LAS u64*)(wp + i * 8) = pack4(((qd[4 * i] * f0 + qd[4 * i + 1] * f1) + (qd[4 * i + 2] * f2 + qd[4 * i + 3] * f3)) * rl);
    }
    LBAR();
    { u32x4 oa[6], ob[3], od[2];
#pragma unroll
      for (int i = 0; i < 6; ++i) oa[i] = *(lu)(cp + i * 1024);
#pragma unroll
      for (int i = 0; i < 3; ++i) ob[i] = *(lu)(lp + X_ST + grp * TILEB + par * 3072 + i * 1024);
      if (extra) { od[0] = *(lu)(lp + X_ST + 2 * TILEB + wid * 2048); od[1] = *(lu)(lp + X_ST + 2 * TILEB + wid * 2048 + 1024); }
      SBAR();
      if (!bada) { char* op = (char*)(out + (size_t)rowa * CH) + lane * 16;
#pragma unroll
          for (int i = 0; i < 6; ++i) *(u32x4*)(op + i * 1024) = oa[i]; }
      if (okb) { char* op = (char*)(out + (size_t)rowb * CH) + par * 3072 + lane * 16;
#pragma unroll
          for (int i = 0; i < 3; ++i) *(u32x4*)(op + i * 1024) = ob[i]; }
      if (extra && okd && wid < 3) { char* op = (char*)(out + (size_t)rowd * CH) + wid * 2048 + lane * 16; *(u32x4*)op = od[0]; *(u32x4*)(op + 1024) = od[1]; } }
    if (__builtin_expect(bada || badb || badd, 0)) {
        __builtin_amdgcn_fence(__ATOMIC_ACQUIRE, "agent");
        for (int k = 0; k < 3; ++k) { const int row = k == 0 ? rowa : (k == 1 ? rowb : rowd);
            if (k == 0 ? bada : (k == 1 ? (badb && par == 0) : (badd && wid == 1))) slow_tile(x, img, out, row, lane); }
    }
    STAMP(7);
}

__global__ __launch_bounds__(256, 1) void fused_kernel(const uint16_t* __restrict__ x, const uint16_t* __restrict__ wq, const uint16_t* __restrict__ wkv,
                                                       const uint16_t* __restrict__ wo, char* ws, uint16_t* __restrict__ out) {
    extern __shared__ __attribute__((aligned(16))) char lds_[];
    const lptr lds = (lptr)lds_;
    const unsigned lds0 = (unsigned)(uintptr_t)lds_;
    const int blk = blockIdx.x, lane = threadIdx.x & 63, wid = __builtin_amdgcn_readfirstlane(threadIdx.x >> 6);
    const int n16 = lane & 15, g4 = lane >> 4;
    char* img = ws;
    gu64* ctl = (gu64*)(ws + CTL_OFF);
    STAMP(0);
    u64 tag;
    asm volatile("s_load_dwordx2 %0, %1, 0x80000" : "=s"(tag) : "s"(ws) : "memory");
    if (wid == 2) asm volatile("global_load_dword a253, %0, %1\n\tglobal_load_dword a254, %2, %3" :: "v"(blk * 36864), "s"(out), "v"(0x60000), "s"(ws) : "memory", "a253", "a254");
    const bool extra = (blk & 7) == ((blk >> 3) & 7);
#define XROWS() do { const char* xr = (const char*)x + (size_t)blk * 36864 + wid * 9216 + lane * 16; \
        _Pragma("unroll") for (int i = 0; i < 9; ++i) glds16(xr + i * 1024, lds0 + L_X + wid * 9216 + i * 1024); \
        const char* xe = (const char*)x + (size_t)(49152 + 32 * (blk >> 3)) * 192 + (wid % 3) * 2048 + lane * 16; \
        glds16(xe, lds0 + L_X + 36864 + (wid % 3) * 2048); glds16(xe + 1024, lds0 + L_X + 36864 + (wid % 3) * 2048 + 1024); } while (0)
    if (blk >= NPREP) XROWS();
    if (blk < NPREP) {
        const int wr = (4 * blk) / NWIN, wc = (4 * blk) % NWIN + wid;
        const uint16_t* src = x + ((size_t)(8 * wr) * IMW + 8 * wc + g4) * CH + 8 * (n16 < 12 ? n16 : 0);
        u32x4 v[16];
#pragma unroll
        for (int i = 0; i < 8; ++i) {
            const uint16_t* s_ = src + i * IMW * CH;
            asm volatile("global_load_dwordx4 %0, %1, off" : "=v"(v[2 * i]) : "v"(s_) : "memory");
            asm volatile("global_load_dwordx4 %0, %1, off offset:768" : "=v"(v[2 * i + 1]) : "v"(s_) : "memory");
        }
        { const char* a = (const char*)wkv + wid * 9216 + lane * 16; const char* c = (const char*)(wid < 2 ? wq : wo) + (wid & 1) * 9216 + lane * 16;
#pragma unroll
          for (int i = 0; i < 9; ++i) glds16(a + i * 1024, lds0 + wid * 9216 + i * 1024);
#pragma unroll
          for (int i = 0; i < 9; ++i) glds16(c + i * 1024, lds0 + 36864 + wid * 9216 + i * 1024); }
        XROWS();
        float s[8] = {};
#define POOL4(q, N) do { asm volatile("s_waitcnt vmcnt(" #N ")" : "+v"(v[4 * q]), "+v"(v[4 * q + 1]), "+v"(v[4 * q + 2]), "+v"(v[4 * q + 3]) :: "memory"); \
        _Pragma("unroll") for (int i = 4 * q; i < 4 * q + 4; ++i) _Pragma("unroll") for (int c = 0; c < 4; ++c) { \
            s[2 * c] += __uint_as_float(v[i][c] << 16); s[2 * c + 1] += __uint_as_float(v[i][c] & 0xFFFF0000u); } } while (0)
        POOL4(0, 41); POOL4(1, 37); POOL4(2, 33); POOL4(3, 29);
#pragma unroll
        for (int c = 0; c < 8; ++c) s[c] = add_x32(add_x16(s[c])) * (1.0f / 64.0f);
        const lptr sxs = lds + L_AUX, skv = lds + L_AUX + 768;
        if (lane < 12) *(LAS u32x4*)(sxs + wid * 192 + 16 * lane) = (u32x4){cvtpk(s[0], s[1]), cvtpk(s[2], s[3]), cvtpk(s[4], s[5]), cvtpk(s[6], s[7])};
        STAMP(1);
        WAIT_BAR(11);
        const int trq = n16 >> 2, trp = n16 & 3;
        f32x4 d[3] = {};
        { const lptr wp = lds + (8 * g4 + trq) * 384 + (48 * wid + 4 * trp) * 2;
#pragma unroll
          for (int ks = 0; ks < 3; ++ks) { const bf16x8 bx = LDF(sxs, (n16 & 3) * 192 + ks * 64 + g4 * 16);
#pragma unroll
              for (int i = 0; i < 3; ++i) d[i] = MFMA16(trfrag(wp + ks * 32 * 384 + i * 32, 4 * 384), bx, d[i]); } }
        bf16x8 w2[9];
        if (wid < 2) {
#pragma unroll
            for (int i = 0; i < 9; ++i) w2[i] = LDF(lds, 36864 + (48 * wid + 16 * (i % 3) + n16) * 192 + (i / 3) * 64 + g4 * 16);
        } else {
            const lptr wp = lds + 55296 + (8 * g4 + trq) * 192 + (48 * (wid - 2) + 4 * trp) * 2;
#pragma unroll
            for (int i = 0; i < 9; ++i) w2[i] = trfrag(wp + (i / 3) * 32 * 192 + (i % 3) * 32, 4 * 192);
        }
        if (n16 < 4) {
#pragma unroll
            for (int i = 0; i < 3; ++i) *(LAS u64*)(skv + n16 * 384 + (48 * wid + 16 * i + 4 * g4) * 2) = pack4(d[i]);
        }
        LBAR();
        f32x4 e[3] = {};
        char* tile = img + (size_t)(blk >> 3) * TILEB;
        if (wid < 2) {
#pragma unroll
            for (int ks = 0; ks < 3; ++ks) { const bf16x8 bx = LDF(skv, (n16 & 3) * 384 + ks * 64 + g4 * 16);
#pragma unroll
                for (int i = 0; i < 3; ++i) e[i] = MFMA16(w2[3 * ks + i], bx, e[i]); }
            if (n16 < 4) {
                char* dst = tile + 3 * wid * 1024 + ((g4 >> 1) * 32 + ((4 * blk + n16) & 31)) * 16 + (g4 & 1) * 8;
#pragma unroll
                for (int i = 0; i < 3; ++i) __hip_atomic_store((gu64*)(dst + i * 1024), pack4(e[i] * QSCALE), RLX_AGENT);
            }
        } else {
#pragma unroll
            for (int ks = 0; ks < 3; ++ks) { const bf16x8 ax = LDF(skv, (n16 & 3) * 384 + 192 + ks * 64 + g4 * 16);
#pragma unroll
                for (int i = 0; i < 3; ++i) e[i] = MFMA16(ax, w2[3 * ks + i], e[i]); }
            if (g4 == 0) {
                const int gk = blk & 7, rho = (n16 & 3) + 4 * (wid - 2) + 8 * (n16 >> 2);
                char* dst = tile + 6144 + (gk >> 2) * 3072 + ((gk & 1) * 32 + rho) * 16 + ((gk >> 1) & 1) * 8;
#pragma unroll
                for (int i = 0; i < 3; ++i) __hip_atomic_store((gu64*)(dst + i * 1024), pack4(e[i]), RLX_AGENT);
            }
        }
    } else if (blk == NPREP) {
        for (int i = threadIdx.x; i < 768; i += 256) { const int pc = i >> 6, l = i & 63;
            char* dst = img + (size_t)24 * TILEB + (pc < 6 ? pc * 1024 + (l >> 5) * 512 + 256 + (l & 31) * 8 : 9216 + (i - 384) * 8);
            __hip_atomic_store((gu64*)dst, 0ull, RLX_AGENT); }
    }
    asm volatile("s_waitcnt vmcnt(0) lgkmcnt(0)\n\ts_barrier" : "+s"(tag) :: "memory");
    tag += 0x9E3779B97F4A7C15ull;
    if (threadIdx.x < NREP) __hip_atomic_store(ctl + 256 + 256 * threadIdx.x + blk, tag, RLX_AGENT);
    STAMP(2);
    if (extra) attend<true>(x, ws, out, lds, lds0, blk, lane, wid, tag); else attend<false>(x, ws, out, lds, lds0, blk, lane, wid, tag);
}

extern "C" void kernel_launch(void* const* d_in, const int* in_sizes, int n_in, void* d_out, int out_size, void* d_ws, size_t ws_size, hipStream_t stream) {
    (void)hipFuncSetAttribute((const void*)fused_kernel, hipFuncAttributeMaxDynamicSharedMemorySize, LDSB);
    fused_kernel<<<GRID, 256, LDSB, stream>>>((const uint16_t*)d_in[0], (const uint16_t*)d_in[1], (const uint16_t*)d_in[2], (const uint16_t*)d_in[3], (char*)d_ws, (uint16_t*)d_out);
}
```

```cpp
#include <hip/hip_runtime.h>
#include <hip/hip_bf16.h>
#include <stdint.h>

typedef __attribute__((ext_vector_type(8))) short bf16x8;
typedef __attribute__((ext_vector_type(4))) short s16x4;
typedef __attribute__((ext_vector_type(16))) float f32x16;
typedef __attribute__((ext_vector_type(4))) float f32x4;
typedef __attribute__((ext_vector_type(2))) float f32x2;
typedef __attribute__((ext_vector_type(2))) __bf16 bf16x2;
typedef __attribute__((ext_vector_type(4))) unsigned u32x4;
typedef __attribute__((ext_vector_type(2))) unsigned u32x2;
#define LAS __attribute__((address_space(3)))
#define GAS __attribute__((address_space(1)))
typedef LAS char* lptr;
typedef GAS unsigned long long gu64;
typedef unsigned long long u64;

constexpr int CH = 96, IMW = 224, NWIN = 28, GRID = 196;
constexpr int TILEB = 12288, NSLOT = 8;
constexpr int XP = 208, L_X = NSLOT * TILEB, L_AUX = L_X + 4 * 64 * XP, LDSB = L_AUX + 4096;
constexpr int CTL_OFF = 1 << 19;
#define NREP 32
constexpr float QSCALE = 0.10206207261596577f * 1.4426950408889634f;

#define MFMA(a, b, c) __builtin_amdgcn_mfma_f32_32x32x16_bf16(a, b, c, 0, 0, 0)
#define MFMA16(a, b, c) __builtin_amdgcn_mfma_f32_16x16x32_bf16(a, b, c, 0, 0, 0)
#define SBAR() __builtin_amdgcn_sched_barrier(0)
#define PIN(x) asm volatile("" : "+v"(x))
#define RLX_AGENT __ATOMIC_RELAXED, __HIP_MEMORY_SCOPE_AGENT
#define WAIT_BAR(N) asm volatile("s_waitcnt vmcnt(" #N ") lgkmcnt(0)\n\ts_barrier" ::: "memory")
#define LBAR() asm volatile("s_waitcnt lgkmcnt(0)\n\ts_barrier" ::: "memory")
#define LDF(p, off) (*(const LAS bf16x8*)((p) + (off)))
#define LDU(p, off) (*(const LAS u32x4*)((p) + (off)))
#define EX2(v) __builtin_amdgcn_exp2f(v)
#define MX3(a, b, c) __builtin_fmaxf(__builtin_fmaxf((a), (b)), (c))
#define PB(P) __builtin_bit_cast(bf16x8, P)
#define ACLOB "a0","a1","a2","a3","a4","a5","a6","a7","a8","a9","a10","a11","a12","a13","a14","a15","a16","a17","a18","a19","a20","a21","a22","a23","a24","a25","a26","a27","a28","a29","a30","a31","a32","a33","a34","a35","a36","a37","a38","a39","a40","a41","a42","a43","a44","a45","a46","a47"
constexpr int XA = 0, XB = 24;

__device__ __forceinline__ unsigned cvtpk(float lo, float hi) { return __builtin_bit_cast(unsigned, __builtin_convertvector((f32x2){lo, hi}, bf16x2)); }
__device__ __forceinline__ bf16x8 pack8(const f32x16& a, int o) {
    u32x4 r = {cvtpk(a[o], a[o + 1]), cvtpk(a[o + 2], a[o + 3]), cvtpk(a[o + 4], a[o + 5]), cvtpk(a[o + 6], a[o + 7])};
    return __builtin_bit_cast(bf16x8, r); }
__device__ __forceinline__ u64 pack4(const f32x4& a) { return (u64)cvtpk(a[0], a[1]) | ((u64)cvtpk(a[2], a[3]) << 32); }
__device__ __forceinline__ float add_x32(float v) { auto r = __builtin_amdgcn_permlane32_swap(__float_as_uint(v), __float_as_uint(v), false, false); return __uint_as_float(r[0]) + __uint_as_float(r[1]); }
__device__ __forceinline__ float add_x16(float v) { auto r = __builtin_amdgcn_permlane16_swap(__float_as_uint(v), __float_as_uint(v), false, false); return __uint_as_float(r[0]) + __uint_as_float(r[1]); }
__device__ __forceinline__ float max_x32(float v) { auto r = __builtin_amdgcn_permlane32_swap(__float_as_uint(v), __float_as_uint(v), false, false); return __builtin_fmaxf(__uint_as_float(r[0]), __uint_as_float(r[1])); }
__device__ __forceinline__ bf16x8 trfrag(lptr p, int rowpitch4) {
    typedef short v4i16_t __attribute__((ext_vector_type(4)));
    const s16x4 lo = __builtin_amdgcn_ds_read_tr16_b64_v4i16((LAS v4i16_t*)p), hi = __builtin_amdgcn_ds_read_tr16_b64_v4i16((LAS v4i16_t*)(p + rowpitch4));
    return (bf16x8){lo[0], lo[1], lo[2], lo[3], hi[0], hi[1], hi[2], hi[3]}; }


__device__ __forceinline__ void slow_tile(const uint16_t* x, const char* img, uint16_t* out, int row, int lane) {
    const int h = lane >> 5; const char* gp = img + lane * 16; const f32x16 zero16 = {};
    bf16x8 xf[6];
#pragma unroll
    for (int i = 0; i < 6; ++i) xf[i] = *(const bf16x8*)(x + (size_t)row * CH + 16 * i + 8 * h);
    float m = -INFINITY, l = 0.f;
    for (int t = 0; t < 25; ++t) {
        f32x16 s = zero16;
#pragma unroll
        for (int i = 0; i < 6; ++i) s = MFMA(*(const bf16x8*)(gp + (size_t)t * TILEB + i * 1024), xf[i], s);
#pragma unroll
        for (int i = 0; i < 16; ++i) if (i < 8 || t < 24) m = __builtin_fmaxf(m, s[i]);
    }
    m = max_x32(m);
    f32x16 o[3] = {zero16, zero16, zero16};
    for (int t = 0; t < 25; ++t) {
        f32x16 s = zero16; const char* tp = gp + (size_t)t * TILEB;
#pragma unroll
        for (int i = 0; i < 6; ++i) s = MFMA(*(const bf16x8*)(tp + i * 1024), xf[i], s);
#pragma unroll
        for (int i = 0; i < 16; ++i) { s[i] = EX2(s[i] - m); if (i < 8 || t < 24) l += s[i]; }
        const bf16x8 q0 = pack8(s, 0), q1 = pack8(s, 8);
#pragma unroll
        for (int c = 0; c < 3; ++c) o[c] = MFMA(*(const bf16x8*)(tp + 6144 + c * 1024), q0, o[c]);
        if (t < 24) {
#pragma unroll
            for (int c = 0; c < 3; ++c) o[c] = MFMA(*(const bf16x8*)(tp + 9216 + c * 1024), q1, o[c]);
        }
    }
    const float rl = 1.0f / add_x32(l);
#pragma unroll
    for (int c = 0; c < 3; ++c) {
#pragma unroll
        for (int i = 0; i < 16; ++i) o[c][i] *= rl;
        uint16_t* op = out + (size_t)row * CH + 48 * h + 16 * c;
        *(bf16x8*)op = pack8(o[c], 0); *(bf16x8*)(op + 8) = pack8(o[c], 8);
    }
}

__global__ __launch_bounds__(256, 1) void fused_kernel(const uint16_t* __restrict__ x, const uint16_t* __restrict__ wq, const uint16_t* __restrict__ wkv,
                                                       const uint16_t* __restrict__ wo, char* ws, uint16_t* __restrict__ out) {
    extern __shared__ __attribute__((aligned(16))) char lds_[];
    const lptr lds = (lptr)lds_;
    const unsigned lds0 = (unsigned)(uintptr_t)lds_;
    const int blk = blockIdx.x, lane = threadIdx.x & 63, wid = __builtin_amdgcn_readfirstlane(threadIdx.x >> 6);
    const int r = lane & 31, h = lane >> 5, n16 = lane & 15, g4 = lane >> 4;
    char* img = ws;
    gu64* ctl = (gu64*)(ws + CTL_OFF);
    u64 tag;
    asm volatile("s_load_dwordx2 %0, %1, 0x80000" : "=s"(tag) : "s"(ws) : "memory");
#define M0SET(v) asm volatile("s_mov_b32 m0, %0" :: "s"(v) : "memory")
    { const int pi = (blk >> 3) % 24;
      const char* pb_ = pi < 12 ? (const char*)wkv + pi * 3072 : pi < 18 ? (const char*)wq + (pi - 12) * 3072 : (const char*)wo + (pi - 18) * 3072;
      asm volatile("global_load_dword a63, %0, %1" :: "v"((wid * 6 + (lane < 6 ? lane : 0)) * 128), "s"(pb_) : "memory", "a63"); }
    const int wr = (4 * blk) / NWIN, wc = (4 * blk) % NWIN + wid;
    u32x4 v[16];
    const char* src = (const char*)(x + ((size_t)(8 * wr) * IMW + 8 * wc) * CH);
    const unsigned xo = g4 * 192 + 16 * (n16 < 12 ? n16 : 0);
#define XB_ " nt"
#define XL(i) asm volatile("global_load_dwordx4 %0, %2, %3" XB_ "\n\tglobal_load_dwordx4 %1, %2, %3 offset:768" XB_ : "=&v"(v[2 * i]), "=&v"(v[2 * i + 1]) : "v"(xo), "s"(src + i * IMW * CH * 2) : "memory")
    XL(0); XL(1);
    XL(2); XL(3);
    unsigned w1o[3], w2o[3];
#pragma unroll
    for (int k = 0; k < 3; ++k) { const int q48 = ((64 * k + lane) * 171) >> 13;
        w1o[k] = (lane * 16) ^ (q48 << 5); w2o[k] = wid < 2 ? 2048 + n16 * 192 + g4 * 16 + k * 64 - k * 1024 : 2048 + ((lane * 16) ^ ((q48 & 1) << 5)); }
    const char* w1g = (const char*)wkv + wid * 9216; const char* w2g = (const char*)(wid < 2 ? wq : wo) + (wid & 1) * 9216 - 2048;
#define WDMA(vo_, g_, l_) do { M0SET(l_); asm volatile("s_nop 0\n\tglobal_load_lds_dwordx4 %0, %3\n\tglobal_load_lds_dwordx4 %1, %3 offset:1024\n\tglobal_load_lds_dwordx4 %2, %3 offset:2048" \
        :: "v"(vo_[0]), "v"(vo_[1]), "v"(vo_[2]), "s"(g_) : "memory"); } while (0)
#define W1(g) WDMA(w1o, w1g + g * 3072, lds0 + wid * 9216 + g * 3072)
#define W2(g) WDMA(w2o, w2g + g * 3072, lds0 + 36864 + wid * 9216 + g * 3072)
    {
    float s[8] = {};
#define POOL2(q, N) do { asm volatile("s_waitcnt vmcnt(" #N ")" : "+v"(v[2 * q]), "+v"(v[2 * q + 1]) :: "memory"); \
    _Pragma("unroll") for (int i = 2 * q; i < 2 * q + 2; ++i) _Pragma("unroll") for (int c = 0; c < 4; ++c) { \
        s[2 * c] += __uint_as_float(v[i][c] << 16); s[2 * c + 1] += __uint_as_float(v[i][c] & 0xFFFF0000u); } \
    asm volatile("" : "+v"(s[0]), "+v"(s[1]), "+v"(s[2]), "+v"(s[3]), "+v"(s[4]), "+v"(s[5]), "+v"(s[6]), "+v"(s[7])); } while (0)
    W1(0); W1(1); W1(2); W2(0); W2(1); W2(2);
    XL(4); XL(5); XL(6); XL(7); POOL2(0, 32); POOL2(1, 30); POOL2(2, 28); POOL2(3, 26); WAIT_BAR(8);
#define PREST() POOL2(4, 6); POOL2(5, 4); POOL2(6, 2); POOL2(7, 0)
    const int trq = n16 >> 2, trp = n16 & 3;
    bf16x8 w1[9], w2[9];
    { const lptr wp = lds + (4 * g4 + trq) * 384 + 8 * trp; const int f = (2 * g4 + (trq >> 1)) & 3;
#pragma unroll
      for (int i = 0; i < 9; ++i) w1[i] = trfrag(wp + (((3 * wid + i % 3) ^ f) << 5) + (i / 3) * 32 * 384, 16 * 384); }
    if (wid < 2) {
#pragma unroll
        for (int i = 0; i < 9; ++i) w2[i] = LDF(lds, 36864 + wid * 9216 + ((i % 3) * 3 + i / 3) * 1024 + lane * 16);
    } else {
        const lptr wp = lds + 55296 + (4 * g4 + trq) * 192 + 8 * trp;
#pragma unroll
        for (int i = 0; i < 9; ++i) w2[i] = trfrag(wp + (((3 * (wid - 2) + i % 3) ^ (g4 & 1)) << 5) + (i / 3) * 32 * 192, 16 * 192);
    }
    const lptr sxs = lds + L_AUX, skv = lds + L_AUX + 768;
    lptr pxs = sxs + wid * 192 + (n16 >> 2) * 64 + (n16 & 1) * 32 + ((n16 >> 1) & 1) * 8 + (g4 >> 1) * 16 + (g4 & 1) * 4, rxs = sxs + (n16 & 3) * 192 + g4 * 16;
    lptr pkv = skv + n16 * 384 + (g4 << (3 + (wid >> 1))) + 96 * wid, rkv = skv + (n16 & 3) * 384 + (wid >> 1) * 192 + g4 * 16;
    char* tile = img + (size_t)(blk >> 3) * TILEB;
    const int gk = blk & 7;
    unsigned dso = wid < 2 ? 3 * wid * 1024 + ((g4 >> 1) * 32 + ((4 * blk + n16) & 31)) * 16 + (g4 & 1) * 8
                           : 6144 + (gk >> 2) * 3072 + ((gk & 1) * 32 + (n16 & 3) + 4 * (wid - 2) + 8 * (n16 >> 2)) * 16 + ((gk >> 1) & 1) * 8;
    PIN(pxs); PIN(rxs); PIN(pkv); PIN(rkv); PIN(dso);
    SBAR();
    PREST();
    { float t[4], u[2];
#pragma unroll
      for (int c = 0; c < 4; ++c) { auto q = __builtin_amdgcn_permlane32_swap(__float_as_uint(s[c]), __float_as_uint(s[c + 4]), false, false); t[c] = __uint_as_float(q[0]) + __uint_as_float(q[1]); }
#pragma unroll
      for (int c = 0; c < 2; ++c) { auto q = __builtin_amdgcn_permlane16_swap(__float_as_uint(t[c]), __float_as_uint(t[c + 2]), false, false); u[c] = (__uint_as_float(q[0]) + __uint_as_float(q[1])) * (1.0f / 64.0f); }
      if (n16 < 12) *(LAS unsigned*)pxs = cvtpk(u[0], u[1]); }
    LBAR();
    f32x4 d[3] = {};
    { const bf16x8 b0 = LDF(rxs, 0), b1 = LDF(rxs, 64), b2 = LDF(rxs, 128); SBAR();
#pragma unroll
      for (int i = 0; i < 9; ++i) d[i % 3] = MFMA16(w1[i], i < 3 ? b0 : i < 6 ? b1 : b2, d[i % 3]); }
    if (n16 < 4) {
#pragma unroll
        for (int i = 0; i < 3; ++i) *(LAS u64*)(pkv + i * 32 - ((3 * wid + i) & (wid >> 1)) * 24) = pack4(d[i]);
    }
    LBAR();
    f32x4 e[3] = {};
    const bf16x8 kb[3] = {LDF(rkv, 0), LDF(rkv, 64), LDF(rkv, 128)};
    SBAR();
    if (wid < 2) {
#pragma unroll
        for (int i = 0; i < 9; ++i) e[i % 3] = MFMA16(w2[i], kb[i / 3], e[i % 3]);
        if (n16 < 4) {
#pragma unroll
            for (int i = 0; i < 3; ++i) __hip_atomic_store((gu64*)(tile + dso + i * 1024), pack4(e[i] * QSCALE), RLX_AGENT);
        }
    } else {
#pragma unroll
        for (int i = 0; i < 9; ++i) e[i % 3] = MFMA16(kb[i / 3], w2[i], e[i % 3]);
        if (g4 == 0) {
#pragma unroll
            for (int i = 0; i < 3; ++i) __hip_atomic_store((gu64*)(tile + dso + i * 1024), pack4(e[i]), RLX_AGENT);
        }
    }
    }
    asm volatile("s_waitcnt vmcnt(0) lgkmcnt(0)\n\ts_barrier" : "+s"(tag) :: "memory");
    tag += 0x9E3779B97F4A7C15ull;
    if (threadIdx.x < NREP) __hip_atomic_store(ctl + 256 + 256 * threadIdx.x + blk, tag, RLX_AGENT);
    const lptr st = lds + L_X + wid * 64 * XP;
    if (n16 < 12) {
#pragma unroll
        for (int i = 0; i < 16; ++i) *(LAS u32x4*)(st + (4 * i + g4) * XP + n16 * 16) = v[i];
    }
    { const unsigned pa = lds0 + L_X + (wid * 64 + r) * XP + 16 * h;
#define XLD(i) asm volatile("ds_read_b128 a[%c1:%c2], %0 offset:%c5\n\tds_read_b128 a[%c3:%c4], %0 offset:%c6" :: "v"(pa), \
          "i"(XA + 4 * i), "i"(XA + 4 * i + 3), "i"(XB + 4 * i), "i"(XB + 4 * i + 3), "i"(32 * i), "i"(32 * i + 32 * XP) : "memory", ACLOB)
      XLD(0); XLD(1); XLD(2); XLD(3); XLD(4); XLD(5);
    }
    f32x16 OA[3] = {}, OB[3] = {};
#pragma unroll
    for (int i = 0; i < 3; ++i) { PIN(OA[i]); PIN(OB[i]); }
    const unsigned vo = lane * 16 + wid * 3072, lk = lds0 + wid * 3072;
#define DMA(g, off) asm volatile("global_load_lds_dwordx4 %0, %1 offset:" #off " sc1" :: "v"(vo), "s"(g) : "memory")
    f32x16 sa, sb, negm;
    u32x4 pa0, pa1, pb0 = {}, pb1 = {}, kf[6], vf[6];
    float la = 0.f, lb = 0.f, la2 = 0.f, lb2 = 0.f;
#pragma unroll
    for (int i = 0; i < 6; ++i) { vf[i] = (u32x4){0, 0, 0, 0}; PIN(vf[i]); }
    PIN(pb0); PIN(pb1);
    if (wid == 0) {
        const GAS u32x4* fp = (const GAS u32x4*)(ctl + 256 + 256 * (blk & (NREP - 1))) + lane, *fq = fp + (lane < 34 ? 64 : 0);
        const unsigned tl = (unsigned)tag, th = (unsigned)(tag >> 32);
        for (unsigned spins = 0; spins < (1u << 20); ++spins) {
            u32x4 f0, f1;
            asm volatile("global_load_dwordx4 %0, %2, off sc1\n\tglobal_load_dwordx4 %1, %3, off sc1\n\ts_waitcnt vmcnt(0)" : "=&v"(f0), "=&v"(f1) : "v"(fp), "v"(fq) : "memory");
            const unsigned df = ((f0[0] ^ tl) | (f0[1] ^ th) | (f0[2] ^ tl) | (f0[3] ^ th)) | ((f1[0] ^ tl) | (f1[1] ^ th) | (f1[2] ^ tl) | (f1[3] ^ th));
            if (__all(df == 0)) break;
        }
    }
    LBAR();
#pragma unroll
    for (int t = 0; t < 6; ++t) { const char* g = img + t * TILEB; M0SET(lk + t * TILEB); asm volatile("s_nop 0"); DMA(g, 0); DMA(g, 1024); DMA(g, 2048); }
    const lptr lp = lds + lane * 16;
    WAIT_BAR(15);
    {
#pragma unroll
      for (int i = 0; i < 6; ++i) kf[i] = LDU(lp, i * 1024);
      asm volatile("v_mfma_f32_32x32x16_bf16 %0, %1, a[0:3], 0\n\tv_mfma_f32_32x32x16_bf16 %0, %2, a[4:7], %0\n\tv_mfma_f32_32x32x16_bf16 %0, %3, a[8:11], %0\n\t"
                   "v_mfma_f32_32x32x16_bf16 %0, %4, a[12:15], %0\n\tv_mfma_f32_32x32x16_bf16 %0, %5, a[16:19], %0\n\tv_mfma_f32_32x32x16_bf16 %0, %6, a[20:23], %0\n\t"
                   "s_nop 15\n\ts_nop 3"
                   : "=&v"(sa) : "v"(kf[0]), "v"(kf[1]), "v"(kf[2]), "v"(kf[3]), "v"(kf[4]), "v"(kf[5]) : "memory", ACLOB);
      float a = MX3(sa[0], sa[1], sa[2]), c = MX3(sa[3], sa[4], sa[5]);
      a = MX3(a, sa[6], sa[7]); c = MX3(c, sa[8], sa[9]); a = MX3(a, sa[10], sa[11]); c = MX3(c, sa[12], sa[13]); a = MX3(a, sa[14], sa[15]);
      const float m = max_x32(__builtin_fmaxf(a, c));
#pragma unroll
      for (int i = 0; i < 16; ++i) { negm[i] = -m; sa[i] -= m; } }
    if (blk == 0 && threadIdx.x == 0) __hip_atomic_store(ctl, tag, RLX_AGENT);
    asm volatile("s_nop 3" : "+v"(negm));
#define E(S, i) S[i] = EX2(S[i])
#define GAP(MF, LD, ...) do { MF; SBAR(); LD; __VA_ARGS__; SBAR(); } while (0)
#define KR(R, i) if (R) kf[i] = LDU(kn, (i) * 1024)
#define VR(R, i) if (R) vf[i] = LDU(vn, 6144 + (i) * 1024)
#define QK0_A(S, i, X) asm volatile("v_mfma_f32_32x32x16_bf16 %0, %1, a[%c3:%c4], %2" : "=&v"(S) : "v"(kf[i]), "v"(negm), "i"(X + 4 * (i)), "i"(X + 4 * (i) + 3) : ACLOB)
#define QKN_A(S, i, X) asm volatile("v_mfma_f32_32x32x16_bf16 %0, %1, a[%c2:%c3], %0" : "+v"(S) : "v"(kf[i]), "i"(X + 4 * (i)), "i"(X + 4 * (i) + 3) : ACLOB)
#define QK0_V(S, i, X) asm volatile("v_mfma_f32_32x32x16_bf16 %0, %1, %2, %3" : "=&v"(S) : "v"(kf[i]), "v"(X[i]), "v"(negm) : ACLOB)
#define QKN_V(S, i, X) asm volatile("v_mfma_f32_32x32x16_bf16 %0, %1, %2, %0" : "+v"(S) : "v"(kf[i]), "v"(X[i]) : ACLOB)
#define QK0(S, i, X, XC) QK0_##XC(S, i, X)
#define QKN(S, i, X, XC) QKN_##XC(S, i, X)
#define PVM(O, i, P) asm volatile("v_mfma_f32_32x32x16_bf16 %0, %1, %2, %0" : "+v"(O[(i) % 3]) : "v"(vf[i]), "v"(P) : ACLOB)
#define X1(S, i) do { e_[i] = EX2(S[i]); PIN(e_[i]); } while (0)
#define CV(P, k, i) do { unsigned c_ = cvtpk(e_[i], e_[(i) + 1]); PIN(c_); P[k] = c_; } while (0)
#define AD(L, i) do { L += e_[i]; PIN(L); } while (0)
#define LGK(n) __builtin_amdgcn_s_waitcnt(0xC07F | ((n) << 8))
#define STEP(SQ, XQ, SC, PC0, PC1, L, L2, OP, PP0, PP1, G0, G1, G2, G3, G4, G5, G6, G7, G8, G9, G10, G11) do { float e_[16]; \
    GAP(QK0_A(SQ, 0, XQ), G0, PIN(SC); X1(SC, 0); X1(SC, 1)); \
    GAP(PVM(OP, 0, PP0), G1, X1(SC, 2); AD(L, 0); AD(L2, 1); CV(PC0, 0, 0)); \
    GAP(QKN_A(SQ, 1, XQ), G2, X1(SC, 3); X1(SC, 4); AD(L, 2)); \
    GAP(PVM(OP, 1, PP0), G3, X1(SC, 5); AD(L2, 3); AD(L, 4); CV(PC0, 1, 2)); \
    GAP(QKN_A(SQ, 2, XQ), G4, X1(SC, 6); AD(L2, 5); CV(PC0, 2, 4)); \
    GAP(PVM(OP, 2, PP0), G5, X1(SC, 7); X1(SC, 8); AD(L, 6)); \
    GAP(QKN_A(SQ, 3, XQ), G6, X1(SC, 9); AD(L2, 7); AD(L, 8); CV(PC0, 3, 6)); \
    GAP(PVM(OP, 3, PP1), G7, X1(SC, 10); AD(L2, 9); CV(PC1, 0, 8)); \
    GAP(QKN_A(SQ, 4, XQ), G8, X1(SC, 11); X1(SC, 12); AD(L, 10)); \
    GAP(PVM(OP, 4, PP1), G9, X1(SC, 13); AD(L2, 11); AD(L, 12); CV(PC1, 1, 10)); \
    GAP(QKN_A(SQ, 5, XQ), G10, X1(SC, 14); X1(SC, 15); AD(L2, 13)); \
    GAP(PVM(OP, 5, PP1), G11, CV(PC1, 2, 12); AD(L, 14); AD(L2, 15); CV(PC1, 3, 14)); \
    } while (0)
#define DSR(d, p, off) asm volatile("ds_read_b128 %0, %1 offset:%c2" : "=v"(d) : "v"(p), "i"(off) : "memory")
#define KRD(i) DSR(kf[i], kn, KO + (i) * 1024)
#define VRD(i) DSR(vf[i], vn, VO + 6144 + (i) * 1024)
#define WL(n) asm volatile("s_waitcnt lgkmcnt(" #n ")" ::: "memory")
#define M0ADD(v, o) asm volatile("s_add_u32 m0, %0, %c1" :: "s"(v), "i"(o) : "memory", "scc")
#define DMX(vx, off) asm volatile("global_load_lds_dwordx4 %0, %1 offset:" #off " sc1" :: "v"(vx), "s"(g) : "memory")
#define TILE_IT(vx, LO, F8, F9, F10) do { \
    STEP(sb, XB, sa, pa0, pa1, la, la2, OB, pb0, pb1, KRD(0), VRD(0), KRD(1), VRD(1), KRD(2); M0ADD(l, LO), VRD(2), KRD(3), VRD(3), KRD(4), VRD(4), KRD(5), VRD(5); WL(6)); \
    STEP(sa, XA, sb, pb0, pb1, lb, lb2, OA, pa0, pa1, DMX(vx, 0), F8, F9, F10, DMX(vx, 1024), WL(0), , DMX(vx, 2048), F11, F12, , ); } while (0)
    { const unsigned lpu = lds0 + lane * 16, vo2 = vo + TILEB; const char* g = img + 6 * TILEB;
      unsigned l = lk + 6 * TILEB, ro = 0, rn = 2 * TILEB, vn = lpu, kn2;
      int cnt = 12;
      asm volatile("s_waitcnt vmcnt(9)\n\ts_barrier" ::: "memory");
#pragma unroll 1
      do {
#define F11
#define F12
          { const unsigned kn = vn; constexpr int VO = 0, KO = TILEB; TILE_IT(vo, 0, kn2 = lpu + rn; PIN(kn2), asm volatile("s_sub_u32 %0, %0, 1" : "+s"(cnt) :: "scc"), ); }
#undef F11
#undef F12
#define F11 asm volatile("s_waitcnt vmcnt(9)\n\ts_barrier" ::: "memory")
#define F12 do { g += 2 * TILEB; asm volatile("" : "+s"(g)); } while (0)
          { const unsigned kn = kn2; constexpr int VO = TILEB, KO = 0;
            TILE_IT(vo2, TILEB, vn = lpu + rn; PIN(vn), l = lk + ro; ro = rn; asm volatile("" : "+s"(l), "+s"(ro)), rn = (rn + 2 * TILEB) & (rn == 6 * TILEB ? 0 : ~0u); asm volatile("" : "+s"(rn))); }
      } while (cnt != 0); }
#define FIN8(O, k, rl) ({ const f32x16& u_ = O[(k) >> 1]; constexpr int o_ = 8 * ((k) & 1); \
        (u32x4){cvtpk(u_[o_] * rl, u_[o_ + 1] * rl), cvtpk(u_[o_ + 2] * rl, u_[o_ + 3] * rl), cvtpk(u_[o_ + 4] * rl, u_[o_ + 5] * rl), cvtpk(u_[o_ + 6] * rl, u_[o_ + 7] * rl)}; })
    typedef LAS u32x4* lu;
    const lptr wp = st + r * XP + h * 96;
#define LOK(t) __all((t) > 1.0e-18f && (t) < 1.0e18f)
    bool oka, okb;
    { const lptr vn = lp; float e_[16]; u32x4 wf[3];
#define WR(i) wf[i] = LDU(vn, 6144 + (i) * 1024)
#define PVH(O, i, P) asm volatile("v_mfma_f32_32x32x16_bf16 %0, %1, %2, %0" : "+v"(O[i]) : "v"(wf[i]), "v"(P) : ACLOB)
      GAP(QK0_A(sb, 0, XB), WR(0), PIN(sa); X1(sa, 0); X1(sa, 1));
      GAP(PVM(OB, 0, pb0), WR(1), X1(sa, 2); AD(la, 0); AD(la2, 1); CV(pa0, 0, 0));
      GAP(QKN_A(sb, 1, XB), WR(2), X1(sa, 3); X1(sa, 4); AD(la, 2));
      GAP(PVM(OB, 1, pb0), , X1(sa, 5); AD(la2, 3); AD(la, 4); CV(pa0, 1, 2));
      GAP(QKN_A(sb, 2, XB), , X1(sa, 6); X1(sa, 7); AD(la2, 5));
      GAP(PVM(OB, 2, pb0), , CV(pa0, 2, 4); CV(pa0, 3, 6); AD(la, 6); AD(la2, 7); la += la2; PIN(la));
      GAP(QKN_A(sb, 3, XB), , la = add_x32(la); PIN(la));
      GAP(PVH(OA, 0, pa0), , );
      const float ra = __builtin_amdgcn_rcpf(la); oka = LOK(la);
      GAP(QKN_A(sb, 4, XB), , );
      GAP(PVH(OA, 1, pa0), , );
      GAP(QKN_A(sb, 5, XB), , );
      GAP(PVH(OA, 2, pa0), , );
      GAP(PVM(OB, 3, pb1), , PIN(sb); X1(sb, 0); X1(sb, 1); X1(sb, 2));
      GAP(PVM(OB, 4, pb1), , X1(sb, 3); X1(sb, 4); X1(sb, 5));
      GAP(PVM(OB, 5, pb1), , X1(sb, 6); X1(sb, 7); CV(pa1, 0, 0); CV(pa1, 1, 2); CV(pa1, 2, 4); CV(pa1, 3, 6); AD(lb, 0); AD(lb2, 1));
      GAP(PVH(OB, 0, pa1), , AD(lb, 2); AD(lb2, 3); AD(lb, 4); AD(lb2, 5); AD(lb, 6); AD(lb2, 7));
      GAP(PVH(OB, 1, pa1), , lb += lb2; PIN(lb));
      GAP(PVH(OB, 2, pa1), , );
#pragma unroll
      for (int i = 0; i < 3; ++i) PIN(OA[i]);
      *(lu)(wp) = FIN8(OA, 0, ra); *(lu)(wp + 16) = FIN8(OA, 1, ra); *(lu)(wp + 32) = FIN8(OA, 2, ra);
      *(lu)(wp + 48) = FIN8(OA, 3, ra); *(lu)(wp + 64) = FIN8(OA, 4, ra); *(lu)(wp + 80) = FIN8(OA, 5, ra); }
    asm volatile("s_waitcnt vmcnt(0)" ::: "memory");
    const int q12 = (lane * 171) >> 11, c0 = lane + q12, q1 = ((lane + 64) * 171) >> 11, c1 = lane + 64 + q1, q2 = ((lane + 128) * 171) >> 11, c2 = lane + 128 + q2;
    char* ob_ = (char*)(out + ((size_t)(8 * wr) * IMW + 8 * wc) * CH);
    const unsigned o0 = lane * 16, o1 = lane < 32 ? 1024 + lane * 16 : IMW * CH * 2 + (lane - 32) * 16, o2 = IMW * CH * 2 + 512 + lane * 16;
#define OST(p, v) __builtin_nontemporal_store(v, (u32x4*)(p))
#define LOAD6(o_, base) do { _Pragma("unroll") for (int i = 0; i < 2; ++i) { \
      o_[3 * i] = *(lu)(st + c0 * 16 + (base + 16 * i) * XP); o_[3 * i + 1] = *(lu)(st + c1 * 16 + (base + 16 * i) * XP); o_[3 * i + 2] = *(lu)(st + c2 * 16 + (base + 16 * i) * XP); } } while (0)
#define STORE6(o_, base) do { _Pragma("unroll") for (int i = 0; i < 2; ++i) { \
      char* p_ = ob_ + (size_t)(base / 8 + 2 * i) * (IMW * CH * 2); OST(p_ + o0, o_[3 * i]); OST(p_ + o1, o_[3 * i + 1]); OST(p_ + o2, o_[3 * i + 2]); } } while (0)
    { u32x4 oa_[6], ob2_[6];
      LOAD6(oa_, 0);
      SBAR();
#pragma unroll
      for (int i = 0; i < 3; ++i) PIN(OB[i]);
      lb = add_x32(lb); const float rb_ = __builtin_amdgcn_rcpf(lb); okb = LOK(lb); const lptr wq_ = wp + 32 * XP;
      *(lu)(wq_) = FIN8(OB, 0, rb_); *(lu)(wq_ + 16) = FIN8(OB, 1, rb_);
      SBAR();
      if (oka) STORE6(oa_, 0);
      *(lu)(wq_ + 32) = FIN8(OB, 2, rb_); *(lu)(wq_ + 48) = FIN8(OB, 3, rb_); *(lu)(wq_ + 64) = FIN8(OB, 4, rb_); *(lu)(wq_ + 80) = FIN8(OB, 5, rb_);
      LOAD6(ob2_, 32);
      if (okb) STORE6(ob2_, 32); }
    if (__builtin_expect(!(oka && okb), 0)) {
        __builtin_amdgcn_fence(__ATOMIC_ACQUIRE, "agent");
#pragma unroll 1
        for (int k = 0; k < 2; ++k) if (!(k ? okb : oka)) slow_tile(x, img, out, (8 * wr + 4 * k + (r >> 3)) * IMW + 8 * wc + (r & 7), lane);
    }
}

extern "C" void kernel_launch(void* const* d_in, const int* in_sizes, int n_in, void* d_out, int out_size, void* d_ws, size_t ws_size, hipStream_t stream) {
    (void)hipFuncSetAttribute((const void*)fused_kernel, hipFuncAttributeMaxDynamicSharedMemorySize, LDSB);
    fused_kernel<<<GRID, 256, LDSB, stream>>>((const uint16_t*)d_in[0], (const uint16_t*)d_in[1], (const uint16_t*)d_in[2], (const uint16_t*)d_in[3], (char*)d_ws, (uint16_t*)d_out);
}
```
